```python
import torch

def _softmax(x, axis=-1):
    return torch.softmax(x, dim=axis)
def _gelu(x):
    return torch.nn.functional.gelu(x, approximate="tanh")
def _silu(x):
    return torch.nn.functional.silu(x)
def _sigmoid(x):
    return torch.sigmoid(x)
def _layernorm(x, axis=-1, eps=1e-5):
    m = x.mean(dim=axis, keepdim=True)
    v = x.var(dim=axis, keepdim=True, unbiased=False)
    return (x - m) / torch.sqrt(v + eps)
def _scatter_add(tgt, idx, src, axis=0):
    return tgt.clone().index_add_(axis, idx, src)
def _segment_sum(src, seg, n):
    out = torch.zeros((n,) + src.shape[1:], dtype=src.dtype)
    return out.index_add_(0, seg, src)
def _pad(x, pw):
    flat = tuple(v for pair in reversed(pw) for v in pair)
    return torch.nn.functional.pad(x, flat)

kernel_name = 'sequential_preLN__ssm_conv_s4d__mixer_channel_mlp__prompt'

def setup_inputs():

    _w = (lambda a: (lambda t: t.to(torch.bfloat16) if t.is_floating_point() else t)(torch.as_tensor(a)))
    def _A_setup():
        Bsz, L, D, N = 1, 2048, 512, 128
        return {
            "u": _w((lambda *s: torch.randn(*s, dtype=torch.bfloat16) * 0.05)(Bsz, L, D)),
            "log_A_real": _w((lambda *s: torch.randn(*s, dtype=torch.bfloat16) * 3.2)(D, N) * 0.5),
            "A_imag": _w((lambda *s: torch.randn(*s, dtype=torch.bfloat16) * 0.05)(D, N) * 3.14),
            "C_real": _w((lambda *s: torch.randn(*s, dtype=torch.bfloat16) * 0.1)(D, N) / N ** 0.5),
            "C_imag": _w((lambda *s: torch.randn(*s, dtype=torch.bfloat16) * 0.2)(D, N) / N ** 0.5),
            "log_dt": _w((lambda *s: torch.randn(*s, dtype=torch.bfloat16) * 0.05)(D) * 0.5 - 4.0),
            "D_skip": _w((lambda *s: torch.randn(*s, dtype=torch.bfloat16) * 0.05)(D)),
        }
    _A_in = _A_setup()

    def _B_setup():
        B, S, D, F = 1, 2048, 512, 1376
        return {
            "x": _w((lambda *s: torch.randn(*s, dtype=torch.bfloat16) * 0.05)(B, S, D)),
            "w1": _w((lambda *s: torch.randn(*s, dtype=torch.bfloat16) * 1.6)(D, F) * (D ** -0.5)),
            "b1": _w((lambda *s: torch.randn(*s, dtype=torch.bfloat16) * 0.8)(F)),
            "w2": _w((lambda *s: torch.randn(*s, dtype=torch.bfloat16) * 0.05)(F, D) * (F ** -0.5)),
            "b2": _w((lambda *s: torch.randn(*s, dtype=torch.bfloat16) * 0.05)(D)),
        }
    _B_in = _B_setup()

    return {
        "A_u": _A_in["u"],
        "A_log_A_real": _A_in["log_A_real"],
        "A_A_imag": _A_in["A_imag"],
        "A_C_real": _A_in["C_real"],
        "A_C_imag": _A_in["C_imag"],
        "A_log_dt": _A_in["log_dt"],
        "A_D_skip": _A_in["D_skip"],
        "B_x": _B_in["x"],
        "B_w1": _B_in["w1"],
        "B_b1": _B_in["b1"],
        "B_w2": _B_in["w2"],
        "B_b2": _B_in["b2"],
    }

def reference(A_u, A_log_A_real, A_A_imag, A_C_real, A_C_imag, A_log_dt, A_D_skip, B_x, B_w1, B_b1, B_w2, B_b2):
    def _A_ref(u, log_A_real, A_imag, C_real, C_imag, log_dt, D_skip):
        L = u.shape[1]
        dt = torch.exp(log_dt)
        A = -torch.exp(log_A_real) + 1j * A_imag
        C = (C_real + 1j * C_imag) * dt[:, None]
        pos = (lambda x, dt: x.to(dt))(torch.arange(L), log_dt.dtype)
        P = torch.exp((A * dt[:, None])[:, :, None] * pos[None, None, :])
        K = 2.0 * torch.einsum("dn,dnl->dl", C, P).real
        L2 = 2 * L
        u_t = (lambda x, *d: x.permute(*d))(u, 0, 2, 1)
        Kf = (lambda x, *a, **k: torch.fft.rfft(x.float(), *a, **k))(K, L2)
        Uf = (lambda x, *a, **k: torch.fft.rfft(x.float(), *a, **k))(u_t, L2)
        y = (lambda x, *a, **k: torch.fft.irfft(x, *a, **k).to(torch.bfloat16))(Uf * Kf[None, :, :], L2)[:, :, :L]
        y = (lambda x, *d: x.permute(*d))(y, 0, 2, 1)
        return y + u * D_skip[None, None, :]

    def _B_ref(x, w1, b1, w2, b2):
        y = _layernorm(x, axis=-1)
        h = _gelu(torch.einsum("bsd,df->bsf", y, w1) + b1)
        return torch.einsum("bsf,fd->bsd", h, w2) + b2

    x = A_u
    h = x + _A_ref(_layernorm(x, axis=-1), A_log_A_real, A_A_imag, A_C_real, A_C_imag, A_log_dt, A_D_skip)
    h2 = h + _B_ref(_layernorm(h, axis=-1), B_w1, B_b1, B_w2, B_b2)
    return h2
```

```cpp
#include <hip/hip_runtime.h>
#include <hip/hip_bf16.h>
#include <stdint.h>

typedef __attribute__((ext_vector_type(8))) __bf16 bf16x8;
typedef __attribute__((ext_vector_type(2))) __bf16 bf16x2;
typedef __attribute__((ext_vector_type(8))) _Float16 f16x8;
typedef __attribute__((ext_vector_type(2))) _Float16 f16x2;
typedef __attribute__((ext_vector_type(4))) float f32x4;
typedef __attribute__((ext_vector_type(2))) float f32x2;
typedef __attribute__((ext_vector_type(16))) float f32x16;
typedef __attribute__((ext_vector_type(4))) unsigned u32x4;
typedef __attribute__((ext_vector_type(2))) unsigned u32x2;
typedef __attribute__((ext_vector_type(4))) short s16x4;
typedef unsigned short u16;

#define LDS_AS __attribute__((address_space(3)))
#define STAMP(kid, i)
#define SB() __builtin_amdgcn_sched_barrier(0)

constexpr int L_ = 2048, D_ = 512, NS_ = 128, F_ = 1376;
constexpr int KSF = F_ / 32;

constexpr size_t OFF_EP = 0;
constexpr size_t OFF_F1 = 64u << 10;
constexpr size_t OFF_UX = 8u << 20;
constexpr size_t OFF_FL = 128u << 10;
constexpr size_t OFF_HT = 4u << 20;
constexpr size_t OFF_W1 = 13u << 20;
constexpr size_t OFF_W2 = 15u << 20;
constexpr size_t OFF_HH = 18u << 20;
typedef __attribute__((address_space(1))) unsigned gu32;
typedef __attribute__((address_space(1))) unsigned long long gu64;
#define RLX_AGENT __ATOMIC_RELAXED, __HIP_MEMORY_SCOPE_AGENT

constexpr int KCN = 656;
constexpr int KIMG = 2112;
constexpr int KIMG_B = KIMG * 2;

__device__ __forceinline__ unsigned pack2(float a, float b) {
  f32x2 v = {a, b};
  bf16x2 r = __builtin_convertvector(v, bf16x2);
  return __builtin_bit_cast(unsigned, r);
}
__device__ __forceinline__ unsigned pack2h(float a, float b) {
  f32x2 v = {a, b};
  f16x2 r = __builtin_convertvector(v, f16x2);
  return __builtin_bit_cast(unsigned, r);
}
__device__ __forceinline__ float bflo(unsigned u) { return __builtin_bit_cast(float, u << 16); }
__device__ __forceinline__ float bfhi(unsigned u) { return __builtin_bit_cast(float, u & 0xffff0000u); }
__device__ __forceinline__ float bf2f(u16 u) { return __builtin_bit_cast(float, (unsigned)u << 16); }
__device__ __forceinline__ float bfround(float a) { return bflo(pack2(a, a)); }
__device__ __forceinline__ bf16x8 asbf(u32x4 v) { return __builtin_bit_cast(bf16x8, v); }
__device__ __forceinline__ f16x8 ash(u32x4 v) { return __builtin_bit_cast(f16x8, v); }
__device__ __forceinline__ u32x2 trread(const void* p) {
  s16x4 v = __builtin_amdgcn_ds_read_tr16_b64_v4i16((LDS_AS s16x4*)(p));
  return __builtin_bit_cast(u32x2, v);
}
template <int CTRL, int RMASK>
__device__ __forceinline__ float dppf(float idv, float v) {
  return __builtin_bit_cast(float, __builtin_amdgcn_update_dpp(__builtin_bit_cast(int, idv), __builtin_bit_cast(int, v),
                                                               CTRL, RMASK, 0xf, false));
}
__device__ __forceinline__ float wave_sum(float v) {
  v += dppf<0x111, 0xf>(0.f, v);
  v += dppf<0x112, 0xf>(0.f, v);
  v += dppf<0x114, 0xf>(0.f, v);
  v += dppf<0x118, 0xf>(0.f, v);
  v += dppf<0x142, 0xa>(0.f, v);
  v += dppf<0x143, 0xc>(0.f, v);
  return __builtin_bit_cast(float, __builtin_amdgcn_readlane(__builtin_bit_cast(int, v), 63));
}
__device__ __forceinline__ float wave_max(float v) {
  const float ni = -__builtin_inff();
  v = fmaxf(v, dppf<0x111, 0xf>(ni, v));
  v = fmaxf(v, dppf<0x112, 0xf>(ni, v));
  v = fmaxf(v, dppf<0x114, 0xf>(ni, v));
  v = fmaxf(v, dppf<0x118, 0xf>(ni, v));
  v = fmaxf(v, dppf<0x142, 0xa>(ni, v));
  v = fmaxf(v, dppf<0x143, 0xc>(ni, v));
  return __builtin_bit_cast(float, __builtin_amdgcn_readlane(__builtin_bit_cast(int, v), 63));
}

constexpr int UCOPY_B = 4640;
constexpr int K2_KOFF = 0;
constexpr int K2_UOFF = 2 * 2 * KIMG_B;
constexpr int K2_UCH = 4 * UCOPY_B;
constexpr int K2_YOFF = K2_UOFF + 2 * K2_UCH;
constexpr int K2_MOFF = K2_YOFF + 2 * 2048 * 4;
constexpr int K2_TOFF = K2_MOFF + 64;
constexpr int K2_FOFF = K2_TOFF + 8 * 24 * 32 * 4;
constexpr int FSTR = 128 * 8 + 16;
constexpr int F_B1 = 0, F_B2 = 4, F_A1 = 12, F_A2 = 20, F_N = 29;
constexpr int K2_XOFF = K2_FOFF;
constexpr int K2_LDS = K2_FOFF + 2 * F_N * FSTR;

__device__ __forceinline__ f32x2 cmul(f32x2 a, f32x2 b) { return (f32x2){a.x * b.x - a.y * b.y, a.x * b.y + a.y * b.x}; }
__device__ __forceinline__ f32x2 cexp2r(float x, float y) {
  const float e = __builtin_amdgcn_exp2f(x), th = __builtin_amdgcn_fractf(y);
  return (f32x2){e * __builtin_amdgcn_cosf(th), e * __builtin_amdgcn_sinf(th)};
}

struct TOff { u16 v[2064]; };
constexpr TOff make_toff() {
  TOff t{};
  for (int q = 0; q < 2064; ++q) {
    const int lag = q <= 2047 ? 2047 - q : 0;
    const int s = lag >= 1024 ? 3 : lag >= 512 ? 2 : lag >= 256 ? 1 : 0;
    const int pp = s ? ((lag + (1 << (s - 1)) - 1 + ((lag >> s) & 1)) >> s) << s : lag;
    const int r2 = pp >> 8, rp = pp & 7, col = (pp & 255) >> 3;
    const int row = r2 == 0 ? rp : r2 == 1 ? 8 + (rp >> 1) : r2 < 4 ? 8 + 2 * r2 + (rp >> 2) : 12 + r2;
    t.v[q] = (u16)((row * 32 + col) * 4);
  }
  return t;
}
__device__ __attribute__((aligned(16))) const TOff g_toff = make_toff();

template <int TS, int RH>
__device__ __forceinline__ void k2_main(char* smem, char* __restrict__ ws, int ch, int d, float dsk, int lane) {
  constexpr int NF = TS == 0 ? 8 : 6;
  constexpr int NFIN = TS == 0 ? (RH == 0 ? 2 : 1) : (RH == 0 ? 2 : 3);
  constexpr int FIN[3] = {TS == 0 ? (RH == 0 ? 7 : 6) : (RH == 0 ? 5 : 3), TS == 0 ? 2 : (RH == 0 ? 4 : 1), 0};
  constexpr int NSND = TS == 0 ? (RH == 0 ? 1 : 2) : (RH == 0 ? 3 : 2);
  constexpr int SND[3] = {TS == 0 ? (RH == 0 ? 6 : 7) : (RH == 0 ? 3 : 5), TS == 0 ? 2 : (RH == 0 ? 1 : 4), 0};
  const int j = lane & 15, gq = lane >> 4;
  const int tb = 16 * gq + 2 * (j & 3) + 64 * (j >> 2);
  __syncthreads();
  STAMP(1, 2);
  const char* abase = smem + K2_KOFF + ch * (2 * KIMG_B) + ((j & 1) ? 0 : KIMG_B) + 2 * (2072 - 8 * (j >> 1) + 8 * gq) -
                      64 * (4 * RH + 3) - 512 * 7;
  const char* bbase = smem + K2_UOFF + ch * K2_UCH + (j & 3) * UCOPY_B + 2 * (232 + 64 * (j >> 2) + 8 * gq) -
                      64 * (4 * RH + 3);
  f32x4 acc[8] = {};
  bf16x8 A[2][NF], B[2][NF];
#pragma unroll
  for (int a = 0; a < NF; ++a) {
    A[0][a] = *(const bf16x8*)(abase + 512 * (7 - a) + 64 * 3);
    B[0][a] = *(const bf16x8*)(bbase + 512 * a + 64 * 3);
  }
#pragma unroll
  for (int rr = 0; rr < 4; ++rr) {
    if (rr < 3) {
#pragma unroll
      for (int a = 0; a < NF; ++a) {
        A[(rr + 1) & 1][a] = *(const bf16x8*)(abase + 512 * (7 - a) + 64 * (2 - rr));
        B[(rr + 1) & 1][a] = *(const bf16x8*)(bbase + 512 * a + 64 * (2 - rr));
      }
    }
    SB();
#pragma unroll
    for (int a = 0; a < NF; ++a)
#pragma unroll
      for (int b = 0; b < NF; ++b) {
        const int nt = a + b;
        const bool use = TS == 0 ? (nt == 7 || nt == 6 || nt == 2) : (nt <= 5 && nt != 2);
        if (use) acc[nt] = __builtin_amdgcn_mfma_f32_16x16x32_bf16(A[rr & 1][a], B[rr & 1][b], acc[nt], 0, 0, 0);
      }
    SB();
  }
  STAMP(1, 3);
  float* Y = (float*)(smem + K2_YOFF) + ch * 2048;
#pragma unroll
  for (int n = 0; n < NSND; ++n) *(f32x4*)(Y + SND[n] * 256 + lane * 4) = acc[SND[n]];
  __syncthreads();
  const unsigned* ulds = (const unsigned*)(smem + K2_UOFF + ch * K2_UCH + 3 * UCOPY_B);
  const unsigned* xlds = (const unsigned*)(smem + K2_XOFF + ch * 4096);
  unsigned* hT = (unsigned*)((u16*)(ws + OFF_HT) + (size_t)d * L_);
#pragma unroll
  for (int n = 0; n < NFIN; ++n) {
    const f32x4 o = *(const f32x4*)(Y + FIN[n] * 256 + lane * 4);
    const f32x4 m = acc[FIN[n]];
    const int t0 = 256 * FIN[n] + tb;
#pragma unroll
    for (int hf = 0; hf < 2; ++hf) {
      const unsigned up = ulds[(t0 + 8 * hf + 200) >> 1];
      const unsigned yb = pack2(m[2 * hf] + o[2 * hf], m[2 * hf + 1] + o[2 * hf + 1]);
      const unsigned sk = pack2(bflo(up) * dsk, bfhi(up) * dsk);
      const unsigned s = pack2(bflo(yb) + bflo(sk), bfhi(yb) + bfhi(sk));
      const unsigned xp = xlds[(t0 + 8 * hf) >> 1];
      hT[(t0 + 8 * hf) >> 1] = pack2(bflo(xp) + bflo(s), bfhi(xp) + bfhi(s));
    }
  }
}

__global__ __launch_bounds__(512) void k2_conv(const u16* __restrict__ x, const u16* __restrict__ la,
                                               const u16* __restrict__ ai, const u16* __restrict__ cr,
                                               const u16* __restrict__ ci, const u16* __restrict__ ldt,
                                               const u16* __restrict__ dskip, const u16* __restrict__ w1,
                                               const u16* __restrict__ w2, char* __restrict__ ws) {
  __shared__ __attribute__((aligned(16))) char smem[K2_LDS];
  const int t = threadIdx.x, lane = t & 63;
  const int w = __builtin_amdgcn_readfirstlane(t >> 6);
  const int blk = blockIdx.x;
  const int ch = w >> 2;
  const int d = 2 * blk + ch;
  float* SC = (float*)(smem + K2_MOFF);
  float* T = (float*)(smem + K2_TOFF);
  STAMP(1, 0);
  u32x4 xrow[2];
#pragma unroll
  for (int i = 0; i < 2; ++i) xrow[i] = *(const u32x4*)(x + (size_t)(16 * (blk >> 1) + 2 * w + i) * D_ + lane * 8);
  const unsigned epoch = __hip_atomic_load((gu32*)(ws + OFF_EP), RLX_AGENT);
  const int half = (w >> 1) & 1, n = t & 127;
  const int pi = d * NS_ + n;
  const u16 p_la = la[pi], p_ai = ai[pi], p_dt = ldt[d];
  u16 p_cr = 0, p_ci = 0;
  if (half == 0) { p_cr = cr[pi]; p_ci = ci[pi]; }
  const int c = t >> 8, v = t & 255;
  const float dsk = bf2f(dskip[d]);
  const u32x4 tof = *(const u32x4*)(g_toff.v + 8 * v);
  const unsigned tof8 = g_toff.v[8 * v + 8];
  const int gw = blk * 8 + w;
  u32x4 wl[2];
  if (gw < 1376) {
    const u16* src;
    int rs;
    if (gw < 688) { src = w1 + (size_t)(32 * (gw & 15)) * F_ + 32 * (gw >> 4); rs = F_; }
    else { const int u2 = gw - 688, ap = u2 / KSF, kb = u2 - ap * KSF; src = w2 + (size_t)(32 * kb) * D_ + 32 * ap; rs = D_; }
    src += (lane >> 2) * rs + (lane & 3) * 8;
    wl[0] = *(const u32x4*)src;
    wl[1] = *(const u32x4*)(src + 16 * rs);
  }
  const int role = w & 3;
  SB();
  const unsigned tag = __builtin_amdgcn_readfirstlane(epoch) + 1u;
  const unsigned long long fval = ((unsigned long long)tag << 32) | (unsigned)~tag;
  const int g4 = lane >> 4, qq = (lane & 15) >> 2, p4 = lane & 3;
  {
    float s1[2], s2[2];
#pragma unroll
    for (int i = 0; i < 2; ++i) {
      s1[i] = 0.f; s2[i] = 0.f;
#pragma unroll
      for (int j = 0; j < 4; ++j) {
        const float a = bflo(xrow[i][j]), b = bfhi(xrow[i][j]);
        s1[i] += a + b;
        s2[i] += a * a + b * b;
      }
    }
#pragma unroll
    for (int i = 0; i < 2; ++i) { s1[i] = wave_sum(s1[i]) * (1.0f / D_); s2[i] = wave_sum(s2[i]) * (1.0f / D_); }
    char* S = smem + K2_TOFF;
    if ((lane >> 5) == (blk & 1)) {
#pragma unroll
      for (int i = 0; i < 2; ++i) {
        const float rs = __builtin_amdgcn_rsqf(fmaxf(s2[i] - s1[i] * s1[i], 0.0f) + 1e-5f);
        u32x4 uv;
#pragma unroll
        for (int j = 0; j < 4; ++j) uv[j] = pack2((bflo(xrow[i][j]) - s1[i]) * rs, (bfhi(xrow[i][j]) - s1[i]) * rs);
        *(u32x4*)(S + (2 * w + i) * 544 + (lane & 31) * 16) = uv;
        *(u32x4*)(S + 8704 + (2 * w + i) * 544 + (lane & 31) * 16) = xrow[i];
      }
    }
    __syncthreads();
    const __amdgpu_buffer_rsrc_t rux = __builtin_amdgcn_make_buffer_rsrc(ws + OFF_UX, 0, D_ * 8192, 0x00020000);
    const char* a = S + (g4 >> 1) * 8704 + (8 * (g4 & 1) + qq) * 544 + (32 * w + 4 * p4) * 2;
#pragma unroll
    for (int r = 0; r < 2; ++r) {
      const u32x2 v0 = trread(a + r * 32), v1 = trread(a + r * 32 + 4 * 544);
      const int dd = 256 * (blk & 1) + 32 * w + 16 * r + (lane & 15);
      __builtin_amdgcn_raw_buffer_store_b128((u32x4){v0[0], v0[1], v1[0], v1[1]}, rux, dd * 8192 + (blk >> 1) * 64 + g4 * 16, 0, 16);
    }
  }
  STAMP(1, 9);
  {
    const float dt = bfround(__builtin_amdgcn_exp2f(bf2f(p_dt) * 1.4426950408889634f));
    const float are = -bfround(__builtin_amdgcn_exp2f(bf2f(p_la) * 1.4426950408889634f));
    const float ax = (are * dt) * 1.4426950408889634f, ay = (bf2f(p_ai) * dt) * 0.15915494309189535f;
    char* F = smem + K2_FOFF + ch * (F_N * FSTR) + n * 8;
    if (half == 0) {
      const float Cr = 2.0f * (bf2f(p_cr) * dt), Ci = 2.0f * (bf2f(p_ci) * dt);
      const float mx = wave_max(fmaxf(fabsf(Cr), fabsf(Ci)));
      if (lane == 0) SC[w] = mx;
      const f32x2 z1 = cexp2r(ax, -ay), z8 = cexp2r(8.0f * ax, 8.0f * ay);
      const f32x2 z16 = cmul(z8, z8);
      *(f32x2*)(F + (F_B1 + 0) * FSTR) = (f32x2){1.0f, 0.0f};
      *(f32x2*)(F + (F_B1 + 1) * FSTR) = z8;
      *(f32x2*)(F + (F_B1 + 2) * FSTR) = z16;
      *(f32x2*)(F + (F_B1 + 3) * FSTR) = cmul(z16, z8);
      f32x2 a1[8];
      a1[0] = (f32x2){Cr, -Ci};
      const f32x2 z2 = cmul(z1, z1), z4 = cmul(z2, z2);
      a1[1] = cmul(a1[0], z1); a1[2] = cmul(a1[0], z2); a1[3] = cmul(a1[1], z2);
#pragma unroll
      for (int k = 0; k < 4; ++k) a1[4 + k] = cmul(a1[k], z4);
      __syncthreads();
      const unsigned eb = min((__builtin_bit_cast(unsigned, fmaxf(SC[4 * ch], SC[4 * ch + 1])) >> 23) + 2u, 252u);
      const float inv = __builtin_bit_cast(float, (254u - eb) << 23);
#pragma unroll
      for (int k = 0; k < 8; ++k) *(f32x2*)(F + (F_A1 + k) * FSTR) = a1[k] * inv;
    } else {
      const f32x2 y1 = cexp2r(32.0f * ax, 32.0f * ay), x1 = cexp2r(256.0f * ax, -256.0f * ay);
      const f32x2 y2 = cmul(y1, y1), y4 = cmul(y2, y2), y3 = cmul(y2, y1);
      *(f32x2*)(F + (F_B2 + 0) * FSTR) = (f32x2){1.0f, 0.0f};
      *(f32x2*)(F + (F_B2 + 1) * FSTR) = y1;
      *(f32x2*)(F + (F_B2 + 2) * FSTR) = y2;
      *(f32x2*)(F + (F_B2 + 3) * FSTR) = y3;
      *(f32x2*)(F + (F_B2 + 4) * FSTR) = y4;
      *(f32x2*)(F + (F_B2 + 5) * FSTR) = cmul(y4, y1);
      *(f32x2*)(F + (F_B2 + 6) * FSTR) = cmul(y4, y2);
      *(f32x2*)(F + (F_B2 + 7) * FSTR) = cmul(y4, y3);
      const f32x2 x2 = cmul(x1, x1), x4 = cmul(x2, x2), x3 = cmul(x2, x1);
      *(f32x2*)(F + (F_A2 + 0) * FSTR) = (f32x2){1.0f, 0.0f};
      *(f32x2*)(F + (F_A2 + 1) * FSTR) = x1;
      *(f32x2*)(F + (F_A2 + 2) * FSTR) = x2;
      *(f32x2*)(F + (F_A2 + 3) * FSTR) = x3;
      *(f32x2*)(F + (F_A2 + 4) * FSTR) = x4;
      *(f32x2*)(F + (F_A2 + 5) * FSTR) = cmul(x4, x1);
      *(f32x2*)(F + (F_A2 + 6) * FSTR) = cmul(x4, x2);
      *(f32x2*)(F + (F_A2 + 7) * FSTR) = cmul(x4, x3);
      *(f32x2*)(F + (F_A2 + 8) * FSTR) = cmul(x4, x4);
      __syncthreads();
      const int z = (ch << 7) | n;
      if (z < 200) {
        const int cc = z / 100, rem = z - 100 * cc, cix = rem / 25, m = rem - 25 * cix;
        *(u32x4*)(smem + K2_UOFF + cc * K2_UCH + cix * UCOPY_B + m * 16) = (u32x4){0u, 0u, 0u, 0u};
      } else if (z < 232) {
        const int i = z - 200;
        *(u32x4*)(smem + K2_KOFF + (i >> 3) * KIMG_B + (256 + (i & 7)) * 16) = (u32x4){0u, 0u, 0u, 0u};
      }
    }
  }
  asm volatile("s_waitcnt vmcnt(0)" ::: "memory");
  __syncthreads();
  if (t == 0) __hip_atomic_store((gu64*)(ws + OFF_F1) + blk, fval, RLX_AGENT);
  STAMP(1, 5);
  if (gw < 1376) {
    char* S = smem + K2_YOFF + w * 2048;
    *(u32x4*)(S + lane * 16) = wl[0];
    *(u32x4*)(S + 1024 + lane * 16) = wl[1];
    u32x4 o[2];
    char* dst;
    int dstr;
    if (gw < 688) {
#pragma unroll
      for (int f = 0; f < 2; ++f) {
        const char* a = S + (8 * g4 + qq) * 64 + f * 32 + p4 * 8;
        const u32x2 v0 = trread(a), v1 = trread(a + 4 * 64);
        o[f] = (u32x4){v0[0], v0[1], v1[0], v1[1]};
      }
      dst = ws + OFF_W1 + (size_t)((2 * (gw >> 4)) * 16 + (gw & 15)) * 1024;
      dstr = 16 * 1024;
    } else {
#pragma unroll
      for (int f = 0; f < 2; ++f) {
        const char* a = S + (4 * g4 + qq) * 64 + p4 * 16 + f * 8;
        const u32x2 v0 = trread(a), v1 = trread(a + 16 * 64);
        o[f] = (u32x4){v0[0], v0[1], v1[0], v1[1]};
      }
      const int u2 = gw - 688, ap = u2 / KSF, kb = u2 - ap * KSF;
      dst = ws + OFF_W2 + (size_t)((2 * ap) * KSF + kb) * 1024;
      dstr = KSF * 1024;
    }
    *(u32x4*)(dst + lane * 16) = o[0];
    *(u32x4*)(dst + dstr + lane * 16) = o[1];
  }
  {
    const int kq = w & 3;
    const int r = lane & 31, hh = lane >> 5;
    const int r1 = r < 8 ? r : r < 12 ? 2 * (r - 8) : r < 16 ? 4 * (r & 1) : 0;
    const int r2 = r < 8 ? 0 : r < 12 ? 1 : r < 16 ? 2 + ((r - 12) >> 1) : r < 21 ? r - 12 : 0;
    const char* F = smem + K2_FOFF + ch * (F_N * FSTR) + (kq * 32 + 4 * hh) * 8;
    const char* fb1 = F + (F_B1 + (r & 3)) * FSTR;
    const char* fb2 = F + (F_B2 + (r >> 2)) * FSTR;
    const char* fa1 = F + (F_A1 + r1) * FSTR;
    const char* fa2 = F + (F_A2 + r2) * FSTR;
    f32x16 acc = {};
#pragma unroll
    for (int ks = 0; ks < 4; ++ks) {
      f32x4 b1v[2], b2v[2], a1v[2], a2v[2];
#pragma unroll
      for (int i = 0; i < 2; ++i) {
        b1v[i] = *(const f32x4*)(fb1 + ks * 64 + i * 16);
        b2v[i] = *(const f32x4*)(fb2 + ks * 64 + i * 16);
        a1v[i] = *(const f32x4*)(fa1 + ks * 64 + i * 16);
        a2v[i] = *(const f32x4*)(fa2 + ks * 64 + i * 16);
      }
      u32x4 av, bv;
#pragma unroll
      for (int q = 0; q < 4; ++q) {
        const int i = q >> 1, o = 2 * (q & 1);
        const f32x2 za = cmul((f32x2){a1v[i][o], a1v[i][o + 1]}, (f32x2){a2v[i][o], a2v[i][o + 1]});
        const f32x2 zb = cmul((f32x2){b1v[i][o], b1v[i][o + 1]}, (f32x2){b2v[i][o], b2v[i][o + 1]});
        av[q] = pack2h(za.x, za.y);
        bv[q] = pack2h(zb.x, zb.y);
      }
      acc = __builtin_amdgcn_mfma_f32_32x32x16_f16(ash(av), ash(bv), acc, 0, 0, 0);
    }
    STAMP(1, 7);
    float* Tw = T + w * (24 * 32);
#pragma unroll
    for (int g = 0; g < 12; ++g) Tw[((g & 3) + 8 * (g >> 2) + 4 * hh) * 32 + r] = acc[g];
  }
  STAMP(1, 8);
  if (w == 0) {
    for (unsigned spins = 0; spins < (1u << 20); ++spins) {
      bool ok = true;
#pragma unroll
      for (int i = 0; i < 4; ++i) ok &= __hip_atomic_load((gu64*)(ws + OFF_F1) + lane + 64 * i, RLX_AGENT) == fval;
      if (__all(ok)) break;
      __builtin_amdgcn_s_sleep(1);
    }
  }
  STAMP(1, 6);
  __syncthreads();
  u32x4 ua, ub, xa;
  {
    const __amdgpu_buffer_rsrc_t rux = __builtin_amdgcn_make_buffer_rsrc(ws + OFF_UX + (size_t)blk * 16384, 0, 16384, 0x00020000);
    const int o = (t >> 1) * 64 + (t & 1) * 16;
    ua = __builtin_amdgcn_raw_buffer_load_b128(rux, o, 0, 16);
    xa = __builtin_amdgcn_raw_buffer_load_b128(rux, o + 32, 0, 16);
    ub = __builtin_amdgcn_raw_buffer_load_b128(rux, v < 255 ? o + ((t & 1) ? 48 : 16) : 1 << 20, 0, 16);
  }
  SB();
  {
    const char* Tq = smem + K2_TOFF + c * 4 * (24 * 32 * 4);
    const unsigned eb = min((__builtin_bit_cast(unsigned, fmaxf(SC[4 * c], SC[4 * c + 1])) >> 23) + 2u, 252u);
    const float sc = __builtin_bit_cast(float, eb << 23);
    float kv[9];
#pragma unroll
    for (int jj = 0; jj < 9; ++jj) {
      const char* a = Tq + (jj == 8 ? tof8 : (jj & 1) ? (tof[jj >> 1] >> 16) : (tof[jj >> 1] & 0xffffu));
      kv[jj] = ((*(const float*)a + *(const float*)(a + 3072)) + (*(const float*)(a + 6144) + *(const float*)(a + 9216))) * sc;
    }
    if (v == 255) kv[8] = 0.0f;
    u32x4 c0, c1;
#pragma unroll
    for (int jj = 0; jj < 4; ++jj) {
      c0[jj] = pack2(kv[2 * jj], kv[2 * jj + 1]);
      c1[jj] = pack2(kv[2 * jj + 1], kv[2 * jj + 2]);
    }
    char* dst = smem + K2_KOFF + c * (2 * KIMG_B) + v * 16;
    *(u32x4*)dst = c0;
    *(u32x4*)(dst + KIMG_B) = c1;
  }
  STAMP(1, 10);
  {
    char* ub_ = smem + K2_UOFF + c * K2_UCH + (v + 26) * 16;
    *(u32x4*)(ub_ + 0 * UCOPY_B) = (u32x4){ua[1], ua[2], ua[3], ub[0]};
    *(u32x4*)(ub_ + 1 * UCOPY_B) = (u32x4){ua[2], ua[3], ub[0], ub[1]};
    *(u32x4*)(ub_ + 2 * UCOPY_B) = (u32x4){ua[3], ub[0], ub[1], ub[2]};
    *(u32x4*)(ub_ + 3 * UCOPY_B) = ub;
    if (v == 0) {
      ub_ -= 16;
      *(u32x4*)(ub_ + 0 * UCOPY_B) = (u32x4){0u, 0u, 0u, ua[0]};
      *(u32x4*)(ub_ + 1 * UCOPY_B) = (u32x4){0u, 0u, ua[0], ua[1]};
      *(u32x4*)(ub_ + 2 * UCOPY_B) = (u32x4){0u, ua[0], ua[1], ua[2]};
      *(u32x4*)(ub_ + 3 * UCOPY_B) = ua;
    }
    *(u32x4*)(smem + K2_XOFF + t * 16) = xa;
  }
  STAMP(1, 1);
  if (role == 0) k2_main<0, 0>(smem, ws, ch, d, dsk, lane);
  else if (role == 1) k2_main<0, 1>(smem, ws, ch, d, dsk, lane);
  else if (role == 2) k2_main<1, 0>(smem, ws, ch, d, dsk, lane);
  else k2_main<1, 1>(smem, ws, ch, d, dsk, lane);
  if (blk == 0 && t == 0) __hip_atomic_store((gu32*)(ws + OFF_EP), tag, RLX_AGENT);
  STAMP(1, 4);
}

constexpr int K3_HOFF = 0;
constexpr int K3_XOFF = 65536;
constexpr int K3_ROFF = 0;
constexpr int K3_SOFF = 65536 + 73728;
constexpr int K3_LDS = K3_SOFF + 1024;
#define PF_EARLY 2
#define W1_EARLY 0
#define W1_PFD 2

__device__ __forceinline__ int k3_swz(int row) { return ((row >> 1) & 1) | (((row >> 3) & 1) << 1); }

template <int NT, int NK, int KH>
__device__ __forceinline__ void k34_body(char* smem, const u16* __restrict__ b1, const u16* __restrict__ b2,
                                         char* __restrict__ ws, u16* __restrict__ out, int rt, int fg, int fstart, int w) {
  const int t = threadIdx.x, lane = t & 63;
  const int fq = w & 3;
  const int row0 = rt * 64;
  const int dq = fg;
  const u16* hT = (const u16*)(ws + OFF_HT);
  const int oct = t & 7, dr = t >> 3;
  const int g = lane >> 4, li = lane & 15, qq = li >> 2, p = lane & 3;
  float* SP = (float*)(smem + K3_SOFF);
  const int r0 = 8 * g + qq;
  const char* base0 = smem + K3_HOFF + r0 * 128 + p * 8;
  const int sw0 = k3_swz(r0) * 32;
  f32x4 acc[NT][4] = {};
  f32x4 csv[NT] = {};
  u32x2 bv1[NT];
  const unsigned tag = __builtin_amdgcn_readfirstlane(__hip_atomic_load((gu32*)(ws + OFF_EP), RLX_AGENT));
  u32x4 hres;
  {
    u32x4 hv[8];
#pragma unroll
    for (int i = 0; i < 8; ++i) hv[i] = *(const u32x4*)(hT + (size_t)(dr + 64 * i) * L_ + row0 + 8 * oct);
    const u32x4* W1t = (const u32x4*)(ws + OFF_W1) + lane;
    u32x4 wv[NT][8];
#pragma unroll
    for (int ks = 0; ks < W1_EARLY; ++ks)
#pragma unroll
      for (int n = 0; n < NT; ++n) wv[n][ks] = W1t[((fstart + fq + 4 * n) * 16 + 8 * KH + ks) * 64];
    SB();
    char* hdst = smem + K3_HOFF + dr * 128 + (((oct >> 1) ^ k3_swz(dr)) * 32) + (oct & 1) * 16;
#pragma unroll
    for (int i = 0; i < 8; ++i) *(u32x4*)(hdst + i * (64 * 128)) = hv[i];
    SB();
#pragma unroll
    for (int ks = W1_EARLY; ks < W1_PFD; ++ks)
#pragma unroll
      for (int n = 0; n < NT; ++n) wv[n][ks] = W1t[((fstart + fq + 4 * n) * 16 + 8 * KH + ks) * 64];
    SB();
    STAMP(2, 1);
    __syncthreads();
    STAMP(2, 2);
    const u32x4 ones = {0x3f803f80u, 0x3f803f80u, 0x3f803f80u, 0x3f803f80u};
    f32x4 st0 = {}, st1 = {};
    const char* basew = base0 + ((fq * 32) ^ sw0);
#pragma unroll
    for (int kk = 0; kk < 8; ++kk) {
      const int ks = 8 * KH + kk;
      if (kk + W1_PFD < 8) {
#pragma unroll
        for (int n = 0; n < NT; ++n) wv[n][kk + W1_PFD] = W1t[((fstart + fq + 4 * n) * 16 + ks + W1_PFD) * 64];
      } else if (kk + W1_PFD == 8) {
#pragma unroll
        for (int n = 0; n < NT; ++n) bv1[n] = *(const u32x2*)(b1 + 16 * (fstart + fq + 4 * n) + 4 * g);
      }
      bf16x8 B[4];
#pragma unroll
      for (int lt = 0; lt < 4; ++lt) {
        const char* a = base0 + ks * 4096 + ((lt * 32) ^ sw0);
        const u32x2 v0 = trread(a), v1 = trread(a + 512);
        B[lt] = asbf((u32x4){v0[0], v0[1], v1[0], v1[1]});
      }
      const u32x2 s0 = trread(basew + ks * 4096), s1 = trread(basew + ks * 4096 + 512);
      const bf16x8 fr = asbf((u32x4){s0[0], s0[1], s1[0], s1[1]});
      SB();
      st0 = __builtin_amdgcn_mfma_f32_16x16x32_bf16(asbf(ones), fr, st0, 0, 0, 0);
      st1 = __builtin_amdgcn_mfma_f32_16x16x32_bf16(fr, fr, st1, 0, 0, 0);
#pragma unroll
      for (int n = 0; n < NT; ++n) {
#pragma unroll
        for (int lt = 0; lt < 4; ++lt)
          acc[n][lt] = __builtin_amdgcn_mfma_f32_16x16x32_bf16(asbf(wv[n][kk]), B[lt], acc[n][lt], 0, 0, 0);
        csv[n] = __builtin_amdgcn_mfma_f32_16x16x32_bf16(asbf(wv[n][kk]), asbf(ones), csv[n], 0, 0, 0);
      }
      SB();
    }
    {
      const char* a = base0 + (2 * dq + (w & 1)) * 4096 + (((w >> 1) * 32) ^ sw0);
      const u32x2 v0 = trread(a), v1 = trread(a + 512);
      hres = (u32x4){v0[0], v0[1], v1[0], v1[1]};
    }
    if (g == qq) {
      SP[KH * 128 + 16 * fq + li] = st0[0];
      SP[KH * 128 + 64 + 16 * fq + li] = p == 0 ? st1[0] : p == 1 ? st1[1] : p == 2 ? st1[2] : st1[3];
    }
    f32x4* X = (f32x4*)(smem + K3_XOFF) + w * (9 * 64) + lane;
#pragma unroll
    for (int n = 0; n < NT; ++n) {
      X[(3 * n) * 64] = acc[n][2 * (1 - KH)];
      X[(3 * n + 1) * 64] = acc[n][2 * (1 - KH) + 1];
      X[(3 * n + 2) * 64] = csv[n];
    }
  }
  STAMP(2, 3);
  const u32x4* Ap = (const u32x4*)(ws + OFF_W2) + ((size_t)(4 * dq) * KSF + w) * 64 + lane;
  u32x4 av[NK][4];
#pragma unroll
  for (int i = 0; i < 4; ++i) av[0][i] = Ap[(i * KSF) * 64];
  SB();
  __syncthreads();
  {
    gu64* HH = (gu64*)(ws + OFF_HH);
    const f32x4* X = (const f32x4*)(smem + K3_XOFF) + (w ^ 4) * (9 * 64) + lane;
    float mm[2], ss[2];
#pragma unroll
    for (int h2 = 0; h2 < 2; ++h2) {
      const int l = 16 * (2 * KH + h2) + li;
      const float m = (SP[l] + SP[128 + l]) * (1.0f / D_);
      float var = (SP[64 + l] + SP[192 + l]) * (1.0f / D_) - m * m;
      var = var < 0.f ? 0.f : var;
      const float r1 = __builtin_amdgcn_rsqf(var + 1e-5f);
      const float r2 = __builtin_amdgcn_rsqf(var * r1 * r1 + 1e-5f);
      mm[h2] = m; ss[h2] = r1 * r2;
    }
#pragma unroll
    for (int n = 0; n < NT; ++n) {
      const int T = fstart + fq + 4 * n;
      const float bb[4] = {bflo(bv1[n][0]), bfhi(bv1[n][0]), bflo(bv1[n][1]), bfhi(bv1[n][1])};
      const f32x4 cs = csv[n] + X[(3 * n + 2) * 64];
#pragma unroll
      for (int h2 = 0; h2 < 2; ++h2) {
        const int lt = 2 * KH + h2;
        const f32x4 a2 = acc[n][lt] + X[(3 * n + h2) * 64];
        float o[4];
#pragma unroll
        for (int r = 0; r < 4; ++r) {
          const float xx = ss[h2] * (a2[r] - mm[h2] * cs[r]) + bb[r];
          const float c0 = -2.0f * 0.7978845608028654f * 1.4426950408889634f;
          const float u = xx * __builtin_fmaf(xx * xx, c0 * 0.044715f, c0);
          o[r] = xx * __builtin_amdgcn_rcpf(1.0f + __builtin_amdgcn_exp2f(u));
        }
        const unsigned long long pk = (unsigned long long)pack2(o[0], o[1]) | ((unsigned long long)pack2(o[2], o[3]) << 32);
        __hip_atomic_store(HH + (((size_t)(4 * rt + lt) * KSF + (T >> 1)) * 64 + lane) * 2 + (T & 1), pk, __ATOMIC_RELAXED,
                           __HIP_MEMORY_SCOPE_AGENT);
        const int k = 1 + 2 * n + h2;
        if (k < PF_EARLY && k < NK && k < 2 * NT) {
#pragma unroll
          for (int i = 0; i < 4; ++i) av[k][i] = Ap[(i * KSF + 8 * k) * 64];
        }
        SB();
      }
    }
  }
  STAMP(2, 4);
  asm volatile("s_waitcnt vmcnt(0)" ::: "memory");
  __builtin_amdgcn_s_barrier();
  gu64* flags = (gu64*)(ws + OFF_FL) + rt * (8 * 16);
  const unsigned long long fval = ((unsigned long long)tag << 32) | (unsigned)~tag;
  if (t == 0) __hip_atomic_store(flags + fg * 16, fval, RLX_AGENT);
  STAMP(2, 5);
  constexpr int PFE = (PF_EARLY < NK ? (PF_EARLY < 2 * NT ? PF_EARLY : 2 * NT) : (NK < 2 * NT ? NK : 2 * NT));
#pragma unroll
  for (int k = PFE; k < NK; ++k)
#pragma unroll
    for (int i = 0; i < 4; ++i) av[k][i] = Ap[(i * KSF + 8 * k) * 64];
  const u32x4 bias = *(const u32x4*)(b2 + 64 * dq + 32 * (w & 1) + 8 * g);
  SB();
  if (w == 0) {
    for (unsigned spins = 0; spins < (1u << 22); ++spins) {
      if (__all(__hip_atomic_load(flags + (lane & 7) * 16, RLX_AGENT) == fval)) break;
      __builtin_amdgcn_s_sleep(1);
    }
  }
  __syncthreads();
  STAMP(2, 6);
  f32x4 oc[4][4] = {};
  {
    const __amdgpu_buffer_rsrc_t rs = __builtin_amdgcn_make_buffer_rsrc(ws + OFF_HH, 0, L_ * F_ * 2, 0x00020000);
    const int boff = (((4 * rt) * KSF + w) * 64 + lane) * 16;
    constexpr int NB = 3;
    u32x4 bv[NB][4];
#pragma unroll
    for (int k = 0; k < NB; ++k)
#pragma unroll
      for (int i = 0; i < 4; ++i) bv[k][i] = __builtin_amdgcn_raw_buffer_load_b128(rs, boff + (i * KSF + 8 * k) * 1024, 0, 16);
    SB();
#pragma unroll
    for (int k = 0; k < NK; ++k) {
#pragma unroll
      for (int a = 0; a < 4; ++a)
#pragma unroll
        for (int b = 0; b < 4; ++b)
          oc[a][b] = __builtin_amdgcn_mfma_f32_16x16x32_bf16(asbf(av[k][a]), asbf(bv[k % NB][b]), oc[a][b], 0, 0, 0);
      if (k + NB < NK) {
#pragma unroll
        for (int i = 0; i < 4; ++i)
          bv[k % NB][i] = __builtin_amdgcn_raw_buffer_load_b128(rs, boff + (i * KSF + 8 * (k + NB)) * 1024, 0, 16);
      }
      SB();
    }
  }
  STAMP(2, 7);
  f32x4* R = (f32x4*)(smem + K3_ROFF);
#pragma unroll
  for (int bb = 0; bb < 4; ++bb)
#pragma unroll
    for (int a = 0; a < 4; ++a) R[((w * 4 + bb) * 4 + a) * 64 + lane] = oc[a][bb];
  __syncthreads();
  f32x4 sum[2];
#pragma unroll
  for (int a = 0; a < 2; ++a) {
    const f32x4* Rp = R + (((w >> 1)) * 4 + 2 * (w & 1) + a) * 64 + lane;
    f32x4 s0 = Rp[0 * 1024] + Rp[1 * 1024], s1 = Rp[2 * 1024] + Rp[3 * 1024];
    f32x4 s2 = Rp[4 * 1024] + Rp[5 * 1024], s3 = Rp[6 * 1024] + Rp[7 * 1024];
    sum[a] = (s0 + s1) + (s2 + s3);
  }
  const int lrow = row0 + 16 * (w >> 1) + li;
  u32x4 o;
#pragma unroll
  for (int hf = 0; hf < 2; ++hf) {
#pragma unroll
    for (int k = 0; k < 2; ++k) {
      const unsigned hb = hres[2 * hf + k], bb = bias[2 * hf + k];
      o[2 * hf + k] = pack2(bflo(hb) + (sum[hf][2 * k] + bflo(bb)), bfhi(hb) + (sum[hf][2 * k + 1] + bfhi(bb)));
    }
  }
  *(u32x4*)(out + (size_t)lrow * D_ + 64 * dq + 32 * (w & 1) + 8 * g) = o;
  STAMP(2, 8);
}

__global__ __launch_bounds__(512, 2) void k34_mlp(const u16* __restrict__ b1, const u16* __restrict__ b2,
                                                  char* __restrict__ ws, u16* __restrict__ out) {
  __shared__ __attribute__((aligned(16))) char smem[K3_LDS];
  const int b = blockIdx.x, xcd = b & 7, idx = b >> 3;
  const int rt = 4 * xcd + (idx >> 3), fg = idx & 7;
  const int fstart = fg < 6 ? 11 * fg : 66 + 10 * (fg - 6);
  const int cnt = fg < 6 ? 11 : 10;
  const int w = __builtin_amdgcn_readfirstlane(threadIdx.x >> 6);
  STAMP(2, 0);
  const bool nt3 = (w & 3) + 8 < cnt;
  if (w < 3) {
    if (nt3) k34_body<3, 6, 0>(smem, b1, b2, ws, out, rt, fg, fstart, w);
    else k34_body<2, 6, 0>(smem, b1, b2, ws, out, rt, fg, fstart, w);
  } else if (w == 3) {
    k34_body<2, 5, 0>(smem, b1, b2, ws, out, rt, fg, fstart, w);
  } else {
    if (nt3) k34_body<3, 5, 1>(smem, b1, b2, ws, out, rt, fg, fstart, w);
    else k34_body<2, 5, 1>(smem, b1, b2, ws, out, rt, fg, fstart, w);
  }
}

extern "C" void kernel_launch(void* const* d_in, const int* in_sizes, int n_in, void* d_out, int out_size, void* d_ws,
                              size_t ws_size, hipStream_t stream) {
  const u16* x = (const u16*)d_in[0];
  const u16* la = (const u16*)d_in[1];
  const u16* ai = (const u16*)d_in[2];
  const u16* cr = (const u16*)d_in[3];
  const u16* ci = (const u16*)d_in[4];
  const u16* ldt = (const u16*)d_in[5];
  const u16* dsk = (const u16*)d_in[6];
  const u16* w1 = (const u16*)d_in[8];
  const u16* b1 = (const u16*)d_in[9];
  const u16* w2 = (const u16*)d_in[10];
  const u16* b2 = (const u16*)d_in[11];
  char* ws = (char*)d_ws;
  k2_conv<<<256, 512, 0, stream>>>(x, la, ai, cr, ci, ldt, dsk, w1, w2, ws);
  k34_mlp<<<256, 512, 0, stream>>>(b1, b2, ws, (u16*)d_out);
}
```

```cpp
#include <hip/hip_runtime.h>
#include <hip/hip_bf16.h>
#include <stdint.h>

typedef __attribute__((ext_vector_type(8))) __bf16 bf16x8;
typedef __attribute__((ext_vector_type(2))) __bf16 bf16x2;
typedef __attribute__((ext_vector_type(8))) _Float16 f16x8;
typedef __attribute__((ext_vector_type(2))) _Float16 f16x2;
typedef __attribute__((ext_vector_type(4))) float f32x4;
typedef __attribute__((ext_vector_type(2))) float f32x2;
typedef __attribute__((ext_vector_type(16))) float f32x16;
typedef __attribute__((ext_vector_type(4))) unsigned u32x4;
typedef __attribute__((ext_vector_type(2))) unsigned u32x2;
typedef __attribute__((ext_vector_type(4))) short s16x4;
typedef unsigned short u16;

#define LDS_AS __attribute__((address_space(3)))
#define STAMP(kid, i)
#define SB() __builtin_amdgcn_sched_barrier(0)

constexpr int L_ = 2048, D_ = 512, NS_ = 128, F_ = 1376;
constexpr int KSF = F_ / 32;

constexpr size_t OFF_EP = 0;
constexpr size_t OFF_F1 = 64u << 10;
constexpr size_t OFF_UX = 8u << 20;
constexpr size_t OFF_CS = 256u << 10;
constexpr size_t OFF_FL = 128u << 10;
constexpr size_t OFF_HT = 4u << 20;
constexpr size_t OFF_W1 = 13u << 20;
constexpr size_t OFF_W2 = 15u << 20;
constexpr size_t OFF_HH = 18u << 20;
typedef __attribute__((address_space(1))) unsigned gu32;
typedef __attribute__((address_space(1))) unsigned long long gu64;
#define RLX_AGENT __ATOMIC_RELAXED, __HIP_MEMORY_SCOPE_AGENT

constexpr int KCN = 656;
constexpr int KIMG = 2112;
constexpr int KIMG_B = KIMG * 2;

__device__ __forceinline__ unsigned pack2(float a, float b) {
  f32x2 v = {a, b};
  bf16x2 r = __builtin_convertvector(v, bf16x2);
  return __builtin_bit_cast(unsigned, r);
}
__device__ __forceinline__ unsigned pack2h(float a, float b) {
  f32x2 v = {a, b};
  f16x2 r = __builtin_convertvector(v, f16x2);
  return __builtin_bit_cast(unsigned, r);
}
__device__ __forceinline__ float bflo(unsigned u) { return __builtin_bit_cast(float, u << 16); }
__device__ __forceinline__ float bfhi(unsigned u) { return __builtin_bit_cast(float, u & 0xffff0000u); }
__device__ __forceinline__ float bf2f(u16 u) { return __builtin_bit_cast(float, (unsigned)u << 16); }
__device__ __forceinline__ float bfround(float a) { return bflo(pack2(a, a)); }
__device__ __forceinline__ bf16x8 asbf(u32x4 v) { return __builtin_bit_cast(bf16x8, v); }
__device__ __forceinline__ f16x8 ash(u32x4 v) { return __builtin_bit_cast(f16x8, v); }
__device__ __forceinline__ u32x2 trread(const void* p) {
  s16x4 v = __builtin_amdgcn_ds_read_tr16_b64_v4i16((LDS_AS s16x4*)(p));
  return __builtin_bit_cast(u32x2, v);
}
template <int CTRL, int RMASK>
__device__ __forceinline__ float dppf(float idv, float v) {
  return __builtin_bit_cast(float, __builtin_amdgcn_update_dpp(__builtin_bit_cast(int, idv), __builtin_bit_cast(int, v),
                                                               CTRL, RMASK, 0xf, false));
}
__device__ __forceinline__ float wave_sum(float v) {
  v += dppf<0x111, 0xf>(0.f, v);
  v += dppf<0x112, 0xf>(0.f, v);
  v += dppf<0x114, 0xf>(0.f, v);
  v += dppf<0x118, 0xf>(0.f, v);
  v += dppf<0x142, 0xa>(0.f, v);
  v += dppf<0x143, 0xc>(0.f, v);
  return __builtin_bit_cast(float, __builtin_amdgcn_readlane(__builtin_bit_cast(int, v), 63));
}
__device__ __forceinline__ float wave_max(float v) {
  const float ni = -__builtin_inff();
  v = fmaxf(v, dppf<0x111, 0xf>(ni, v));
  v = fmaxf(v, dppf<0x112, 0xf>(ni, v));
  v = fmaxf(v, dppf<0x114, 0xf>(ni, v));
  v = fmaxf(v, dppf<0x118, 0xf>(ni, v));
  v = fmaxf(v, dppf<0x142, 0xa>(ni, v));
  v = fmaxf(v, dppf<0x143, 0xc>(ni, v));
  return __builtin_bit_cast(float, __builtin_amdgcn_readlane(__builtin_bit_cast(int, v), 63));
}

constexpr int UCOPY_B = 4640;
constexpr int K2_KOFF = 0;
constexpr int K2_UOFF = 2 * 2 * KIMG_B;
constexpr int K2_UCH = 4 * UCOPY_B;
constexpr int K2_YOFF = K2_UOFF + 2 * K2_UCH;
constexpr int K2_MOFF = K2_YOFF + 2 * 2048 * 4;
constexpr int K2_TOFF = K2_MOFF + 64;
constexpr int K2_FOFF = K2_TOFF + 8 * 24 * 32 * 4;
constexpr int FSTR = 128 * 4 + 16;
constexpr int F_B1 = 0, F_B2 = 4, F_A1 = 12, F_A2 = 20, F_N = 29;
constexpr int K2_Y2OFF = K2_FOFF;
constexpr int K2_LDS = K2_FOFF + 32768;

__device__ __forceinline__ f32x2 cmul(f32x2 a, f32x2 b) { return (f32x2){a.x * b.x - a.y * b.y, a.x * b.y + a.y * b.x}; }
__device__ __forceinline__ unsigned cmulh(unsigned a, unsigned b) {
  unsigned t, r;
  asm("v_pk_mul_f16 %0, %1, %2 op_sel:[0,0] op_sel_hi:[1,0]" : "=v"(t) : "v"(a), "v"(b));
  asm("v_pk_fma_f16 %0, %1, %2, %3 op_sel:[1,1,0] op_sel_hi:[0,1,1] neg_lo:[1,0,0]" : "=v"(r) : "v"(a), "v"(b), "v"(t));
  return r;
}
__device__ __forceinline__ f32x2 cexp2r(float x, float y) {
  const float e = __builtin_amdgcn_exp2f(x), th = __builtin_amdgcn_fractf(y);
  return (f32x2){e * __builtin_amdgcn_cosf(th), e * __builtin_amdgcn_sinf(th)};
}

struct TOff { u16 v[2064]; };
constexpr TOff make_toff() {
  TOff t{};
  for (int q = 0; q < 2064; ++q) {
    const int lag = q <= 2047 ? 2047 - q : 0;
    const int s = lag >= 1024 ? 3 : lag >= 512 ? 2 : lag >= 256 ? 1 : 0;
    const int pp = s ? ((lag + (1 << (s - 1)) - 1 + ((lag >> s) & 1)) >> s) << s : lag;
    const int r2 = pp >> 8, rp = pp & 7, col = (pp & 255) >> 3;
    const int row = r2 == 0 ? rp : r2 == 1 ? 8 + (rp >> 1) : r2 < 4 ? 8 + 2 * r2 + (rp >> 2) : 12 + r2;
    t.v[q] = (u16)((row * 32 + col) * 4);
  }
  return t;
}
__device__ __attribute__((aligned(16))) const TOff g_toff = make_toff();

template <int TS>
__device__ __forceinline__ void conv_main(char* smem, int ch, int rh, int lane) {
  constexpr int NF = TS == 0 ? 8 : 6;
  const int j = lane & 15, gq = lane >> 4;
  const char* abase = smem + K2_KOFF + ch * (2 * KIMG_B) + ((j & 1) ? 0 : KIMG_B) + 2 * (2072 - 8 * (j >> 1) + 8 * gq) -
                      64 * (4 * rh + 3) - 512 * 7;
  const char* bbase = smem + K2_UOFF + ch * K2_UCH + (j & 3) * UCOPY_B + 2 * (232 + 64 * (j >> 2) + 8 * gq) -
                      64 * (4 * rh + 3);
  f32x4 acc[8] = {};
  bf16x8 A[2][NF], B[2][NF];
#pragma unroll
  for (int a = 0; a < NF; ++a) {
    A[0][a] = *(const bf16x8*)(abase + 512 * (7 - a) + 64 * 3);
    B[0][a] = *(const bf16x8*)(bbase + 512 * a + 64 * 3);
  }
#pragma unroll
  for (int rr = 0; rr < 4; ++rr) {
    if (rr < 3) {
#pragma unroll
      for (int a = 0; a < NF; ++a) {
        A[(rr + 1) & 1][a] = *(const bf16x8*)(abase + 512 * (7 - a) + 64 * (2 - rr));
        B[(rr + 1) & 1][a] = *(const bf16x8*)(bbase + 512 * a + 64 * (2 - rr));
      }
    }
    SB();
#pragma unroll
    for (int a = 0; a < NF; ++a)
#pragma unroll
      for (int b = 0; b < NF; ++b) {
        const int nt = a + b;
        const bool use = TS == 0 ? (nt == 7 || nt == 6 || nt == 2) : (nt <= 5 && nt != 2);
        if (use) acc[nt] = __builtin_amdgcn_mfma_f32_16x16x32_bf16(A[rr & 1][a], B[rr & 1][b], acc[nt], 0, 0, 0);
      }
    SB();
  }
  float* Y = (float*)(smem + K2_Y2OFF) + (rh * 2 + ch) * 2048 + 16 * gq + 2 * (j & 3) + 64 * (j >> 2);
#pragma unroll
  for (int nt = 0; nt < 8; ++nt) {
    const bool use = TS == 0 ? (nt == 7 || nt == 6 || nt == 2) : (nt <= 5 && nt != 2);
    if (use) {
      *(f32x2*)(Y + 256 * nt) = (f32x2){acc[nt][0], acc[nt][1]};
      *(f32x2*)(Y + 256 * nt + 8) = (f32x2){acc[nt][2], acc[nt][3]};
    }
  }
}

__global__ __launch_bounds__(512) void k2_conv(const u16* __restrict__ x, const u16* __restrict__ la,
                                               const u16* __restrict__ ai, const u16* __restrict__ cr,
                                               const u16* __restrict__ ci, const u16* __restrict__ ldt,
                                               const u16* __restrict__ dskip, const u16* __restrict__ w1,
                                               const u16* __restrict__ w2, char* __restrict__ ws) {
  __shared__ __attribute__((aligned(16))) char smem[K2_LDS];
  const int t = threadIdx.x, lane = t & 63;
  const int w = __builtin_amdgcn_readfirstlane(t >> 6);
  const int blk = blockIdx.x;
  const int ch = w >> 2;
  const int d = 2 * blk + ch;
  float* SC = (float*)(smem + K2_MOFF);
  float* T = (float*)(smem + K2_TOFF);
  STAMP(1, 0);
  u32x4 xrow[2];
#pragma unroll
  for (int i = 0; i < 2; ++i) xrow[i] = *(const u32x4*)(x + (size_t)(16 * (blk >> 1) + 2 * w + i) * D_ + lane * 8);
  const unsigned epoch = __hip_atomic_load((gu32*)(ws + OFF_EP), RLX_AGENT);
  const int half = (w >> 1) & 1, n = t & 127;
  const int pi = d * NS_ + n;
  const u16 p_la = la[pi], p_ai = ai[pi], p_dt = ldt[d];
  u16 p_cr = 0, p_ci = 0;
  if (half == 0) { p_cr = cr[pi]; p_ci = ci[pi]; }
  const int c = t >> 8, v = t & 255;
  const float dsk = bf2f(dskip[2 * blk + (t >> 8)]);
  const u32x4 tof = *(const u32x4*)(g_toff.v + 8 * v);
  const unsigned tof8 = g_toff.v[8 * v + 8];
  const int role = w & 3;
  SB();
  const unsigned tag = __builtin_amdgcn_readfirstlane(epoch) + 1u;
  const unsigned long long fval = ((unsigned long long)tag << 32) | (unsigned)~tag;
  const int g4 = lane >> 4, qq = (lane & 15) >> 2, p4 = lane & 3;
  {
    float s1[2], s2[2];
#pragma unroll
    for (int i = 0; i < 2; ++i) {
      s1[i] = 0.f; s2[i] = 0.f;
#pragma unroll
      for (int j = 0; j < 4; ++j) {
        const float a = bflo(xrow[i][j]), b = bfhi(xrow[i][j]);
        s1[i] += a + b;
        s2[i] += a * a + b * b;
      }
    }
#pragma unroll
    for (int i = 0; i < 2; ++i) { s1[i] = wave_sum(s1[i]) * (1.0f / D_); s2[i] = wave_sum(s2[i]) * (1.0f / D_); }
    if (half == 0) {
      const float mx = wave_max(fmaxf(fabsf(bf2f(p_cr)), fabsf(bf2f(p_ci))));
      if (lane == 0) SC[w] = mx;
    }
    char* S = smem + K2_TOFF;
    if ((lane >> 5) == (blk & 1)) {
#pragma unroll
      for (int i = 0; i < 2; ++i) {
        const float rs = __builtin_amdgcn_rsqf(fmaxf(s2[i] - s1[i] * s1[i], 0.0f) + 1e-5f);
        u32x4 uv;
#pragma unroll
        for (int j = 0; j < 4; ++j) uv[j] = pack2((bflo(xrow[i][j]) - s1[i]) * rs, (bfhi(xrow[i][j]) - s1[i]) * rs);
        *(u32x4*)(S + (2 * w + i) * 544 + (lane & 31) * 16) = uv;
        *(u32x4*)(S + 8704 + (2 * w + i) * 544 + (lane & 31) * 16) = xrow[i];
      }
    }
    __syncthreads();
    const __amdgpu_buffer_rsrc_t rux = __builtin_amdgcn_make_buffer_rsrc(ws + OFF_UX, 0, D_ * 8192, 0x00020000);
    const char* a = S + (g4 >> 1) * 8704 + (8 * (g4 & 1) + qq) * 544 + (32 * w + 4 * p4) * 2;
#pragma unroll
    for (int r = 0; r < 2; ++r) {
      const u32x2 v0 = trread(a + r * 32), v1 = trread(a + r * 32 + 4 * 544);
      const int dd = 256 * (blk & 1) + 32 * w + 16 * r + (lane & 15);
      __builtin_amdgcn_raw_buffer_store_b128((u32x4){v0[0], v0[1], v1[0], v1[1]}, rux, dd * 8192 + (blk >> 1) * 64 + g4 * 16, 0, 16);
    }
  }
  STAMP(1, 9);
  {
    const float dt = bfround(__builtin_amdgcn_exp2f(bf2f(p_dt) * 1.4426950408889634f));
    const float are = -bfround(__builtin_amdgcn_exp2f(bf2f(p_la) * 1.4426950408889634f));
    const float ax = (are * dt) * 1.4426950408889634f, ay = (bf2f(p_ai) * dt) * 0.15915494309189535f;
    char* F = smem + K2_FOFF + ch * (F_N * FSTR) + n * 4;
    auto put = [&](int e, f32x2 z) { *(unsigned*)(F + e * FSTR) = pack2h(z.x, z.y); };
    if (half == 0) {
      const unsigned eb = min((__builtin_bit_cast(unsigned, fmaxf(SC[4 * ch], SC[4 * ch + 1])) >> 23) + 2u, 252u);
      const float inv = __builtin_bit_cast(float, (254u - eb) << 23);
      if (n == 0) SC[8 + ch] = 2.0f * dt * __builtin_bit_cast(float, eb << 23);
      const f32x2 z1 = cexp2r(ax, -ay), z8 = cexp2r(8.0f * ax, 8.0f * ay);
      const f32x2 z16 = cmul(z8, z8);
      put(F_B1 + 0, (f32x2){1.0f, 0.0f});
      put(F_B1 + 1, z8);
      put(F_B1 + 2, z16);
      put(F_B1 + 3, cmul(z16, z8));
      f32x2 a1[8];
      a1[0] = (f32x2){bf2f(p_cr) * inv, -bf2f(p_ci) * inv};
      const f32x2 z2 = cmul(z1, z1), z4 = cmul(z2, z2);
      a1[1] = cmul(a1[0], z1); a1[2] = cmul(a1[0], z2); a1[3] = cmul(a1[1], z2);
#pragma unroll
      for (int k = 0; k < 4; ++k) a1[4 + k] = cmul(a1[k], z4);
#pragma unroll
      for (int k = 0; k < 8; ++k) put(F_A1 + k, a1[k]);
    } else {
      const f32x2 y1 = cexp2r(32.0f * ax, 32.0f * ay), x1 = cexp2r(256.0f * ax, -256.0f * ay);
      const f32x2 y2 = cmul(y1, y1), y4 = cmul(y2, y2), y3 = cmul(y2, y1);
      put(F_B2 + 0, (f32x2){1.0f, 0.0f});
      put(F_B2 + 1, y1); put(F_B2 + 2, y2); put(F_B2 + 3, y3); put(F_B2 + 4, y4);
      put(F_B2 + 5, cmul(y4, y1)); put(F_B2 + 6, cmul(y4, y2)); put(F_B2 + 7, cmul(y4, y3));
      const f32x2 x2 = cmul(x1, x1), x4 = cmul(x2, x2), x3 = cmul(x2, x1);
      put(F_A2 + 0, (f32x2){1.0f, 0.0f});
      put(F_A2 + 1, x1); put(F_A2 + 2, x2); put(F_A2 + 3, x3); put(F_A2 + 4, x4);
      put(F_A2 + 5, cmul(x4, x1)); put(F_A2 + 6, cmul(x4, x2)); put(F_A2 + 7, cmul(x4, x3)); put(F_A2 + 8, cmul(x4, x4));
      const int z = (ch << 7) | n;
      if (z < 200) {
        const int cc = z / 100, rem = z - 100 * cc, cix = rem / 25, m = rem - 25 * cix;
        *(u32x4*)(smem + K2_UOFF + cc * K2_UCH + cix * UCOPY_B + m * 16) = (u32x4){0u, 0u, 0u, 0u};
      } else if (z < 232) {
        const int i = z - 200;
        *(u32x4*)(smem + K2_KOFF + (i >> 3) * KIMG_B + (256 + (i & 7)) * 16) = (u32x4){0u, 0u, 0u, 0u};
      }
    }
  }
  asm volatile("s_waitcnt vmcnt(0)" ::: "memory");
  __syncthreads();
  if (t == 0) __hip_atomic_store((gu64*)(ws + OFF_F1) + blk, fval, RLX_AGENT);
  STAMP(1, 5);
  const int gw = (w >= 1 && w <= 6) ? blk + 256 * (w - 1) : 1376;
  u32x4 wl[2];
  if (gw < 1376) {
    const u16* src;
    int rs;
    if (gw < 688) { src = w1 + (size_t)(32 * (gw & 15)) * F_ + 32 * (gw >> 4); rs = F_; }
    else { const int u2 = gw - 688, ap = u2 / KSF, kb = u2 - ap * KSF; src = w2 + (size_t)(32 * kb) * D_ + 32 * ap; rs = D_; }
    src += (lane >> 2) * rs + (lane & 3) * 8;
    wl[0] = *(const u32x4*)src;
    wl[1] = *(const u32x4*)(src + 16 * rs);
  }
  SB();
  {
    const int kq = w & 3;
    const int r = lane & 31, hh = lane >> 5;
    const int r1 = r < 8 ? r : r < 12 ? 2 * (r - 8) : r < 16 ? 4 * (r & 1) : 0;
    const int r2 = r < 8 ? 0 : r < 12 ? 1 : r < 16 ? 2 + ((r - 12) >> 1) : r < 21 ? r - 12 : 0;
    const char* F = smem + K2_FOFF + ch * (F_N * FSTR) + (kq * 32 + 4 * hh) * 4;
    const char* fb1 = F + (F_B1 + (r & 3)) * FSTR;
    const char* fb2 = F + (F_B2 + (r >> 2)) * FSTR;
    const char* fa1 = F + (F_A1 + r1) * FSTR;
    const char* fa2 = F + (F_A2 + r2) * FSTR;
    f32x16 acc = {};
#pragma unroll
    for (int ks = 0; ks < 4; ++ks) {
      const u32x4 b1v = *(const u32x4*)(fb1 + ks * 32), b2v = *(const u32x4*)(fb2 + ks * 32);
      const u32x4 a1v = *(const u32x4*)(fa1 + ks * 32), a2v = *(const u32x4*)(fa2 + ks * 32);
      u32x4 av, bv;
#pragma unroll
      for (int q = 0; q < 4; ++q) {
        av[q] = cmulh(a1v[q], a2v[q]);
        bv[q] = cmulh(b1v[q], b2v[q]);
      }
      acc = __builtin_amdgcn_mfma_f32_32x32x16_f16(ash(av), ash(bv), acc, 0, 0, 0);
    }
    STAMP(1, 7);
    float* Tw = T + w * (24 * 32);
#pragma unroll
    for (int g = 0; g < 12; ++g) Tw[((g & 3) + 8 * (g >> 2) + 4 * hh) * 32 + r] = acc[g];
  }
  STAMP(1, 8);
  if (w == 0) {
    for (unsigned spins = 0; spins < (1u << 20); ++spins) {
      bool ok = true;
#pragma unroll
      for (int i = 0; i < 4; ++i) ok &= __hip_atomic_load((gu64*)(ws + OFF_F1) + lane + 64 * i, RLX_AGENT) == fval;
      if (__all(ok)) break;
      __builtin_amdgcn_s_sleep(1);
    }
  }
  STAMP(1, 6);
  __syncthreads();
  u32x4 ua, ub, xa;
  {
    const __amdgpu_buffer_rsrc_t rux = __builtin_amdgcn_make_buffer_rsrc(ws + OFF_UX + (size_t)blk * 16384, 0, 16384, 0x00020000);
    const int o = (t >> 1) * 64 + (t & 1) * 16;
    ua = __builtin_amdgcn_raw_buffer_load_b128(rux, o, 0, 16);
    xa = __builtin_amdgcn_raw_buffer_load_b128(rux, o + 32, 0, 16);
    ub = __builtin_amdgcn_raw_buffer_load_b128(rux, v < 255 ? o + ((t & 1) ? 48 : 16) : 1 << 20, 0, 16);
  }
  SB();
  {
    const char* Tq = smem + K2_TOFF + c * 4 * (24 * 32 * 4);
    const float sc = SC[8 + c];
    float kv[9];
#pragma unroll
    for (int jj = 0; jj < 9; ++jj) {
      const char* a = Tq + (jj == 8 ? tof8 : (jj & 1) ? (tof[jj >> 1] >> 16) : (tof[jj >> 1] & 0xffffu));
      kv[jj] = ((*(const float*)a + *(const float*)(a + 3072)) + (*(const float*)(a + 6144) + *(const float*)(a + 9216))) * sc;
    }
    if (v == 255) kv[8] = 0.0f;
    u32x4 c0, c1;
#pragma unroll
    for (int jj = 0; jj < 4; ++jj) {
      c0[jj] = pack2(kv[2 * jj], kv[2 * jj + 1]);
      c1[jj] = pack2(kv[2 * jj + 1], kv[2 * jj + 2]);
    }
    char* dst = smem + K2_KOFF + c * (2 * KIMG_B) + v * 16;
    *(u32x4*)dst = c0;
    *(u32x4*)(dst + KIMG_B) = c1;
  }
  STAMP(1, 10);
  if (gw < 1376) {
    char* S = smem + K2_YOFF + w * 2048;
    *(u32x4*)(S + lane * 16) = wl[0];
    *(u32x4*)(S + 1024 + lane * 16) = wl[1];
    u32x4 o[2];
    int dst, dstr;
    if (gw < 688) {
#pragma unroll
      for (int f = 0; f < 2; ++f) {
        const char* a = S + (8 * g4 + qq) * 64 + f * 32 + p4 * 8;
        const u32x2 v0 = trread(a), v1 = trread(a + 4 * 64);
        o[f] = (u32x4){v0[0], v0[1], v1[0], v1[1]};
      }
      dst = ((2 * (gw >> 4)) * 16 + (gw & 15)) * 1024;
      dstr = 16 * 1024;
      float c2[2];
#pragma unroll
      for (int f = 0; f < 2; ++f) {
        c2[f] = ((bflo(o[f][0]) + bfhi(o[f][0])) + (bflo(o[f][1]) + bfhi(o[f][1]))) +
                ((bflo(o[f][2]) + bfhi(o[f][2])) + (bflo(o[f][3]) + bfhi(o[f][3])));
        c2[f] += __shfl_xor(c2[f], 16);
        c2[f] += __shfl_xor(c2[f], 32);
      }
      if (lane < 32) ((float*)(ws + OFF_CS))[(gw & 15) * F_ + 32 * (gw >> 4) + lane] = lane < 16 ? c2[0] : c2[1];
    } else {
#pragma unroll
      for (int f = 0; f < 2; ++f) {
        const char* a = S + (4 * g4 + qq) * 64 + p4 * 16 + f * 8;
        const u32x2 v0 = trread(a), v1 = trread(a + 16 * 64);
        o[f] = (u32x4){v0[0], v0[1], v1[0], v1[1]};
      }
      const int u2 = gw - 688, ap = u2 / KSF, kb = u2 - ap * KSF;
      dst = (int)(OFF_W2 - OFF_W1) + ((2 * ap) * KSF + kb) * 1024;
      dstr = KSF * 1024;
    }
    const __amdgpu_buffer_rsrc_t rw = __builtin_amdgcn_make_buffer_rsrc(ws + OFF_W1, 0, 4 << 20, 0x00020000);
    __builtin_amdgcn_raw_buffer_store_b128(o[0], rw, dst + lane * 16, 0, 0);
    __builtin_amdgcn_raw_buffer_store_b128(o[1], rw, dst + dstr + lane * 16, 0, 0);
  }
  SB();
  {
    char* ub_ = smem + K2_UOFF + c * K2_UCH + (v + 26) * 16;
    *(u32x4*)(ub_ + 0 * UCOPY_B) = (u32x4){ua[1], ua[2], ua[3], ub[0]};
    *(u32x4*)(ub_ + 1 * UCOPY_B) = (u32x4){ua[2], ua[3], ub[0], ub[1]};
    *(u32x4*)(ub_ + 2 * UCOPY_B) = (u32x4){ua[3], ub[0], ub[1], ub[2]};
    *(u32x4*)(ub_ + 3 * UCOPY_B) = ub;
    if (v == 0) {
      ub_ -= 16;
      *(u32x4*)(ub_ + 0 * UCOPY_B) = (u32x4){0u, 0u, 0u, ua[0]};
      *(u32x4*)(ub_ + 1 * UCOPY_B) = (u32x4){0u, 0u, ua[0], ua[1]};
      *(u32x4*)(ub_ + 2 * UCOPY_B) = (u32x4){0u, ua[0], ua[1], ua[2]};
      *(u32x4*)(ub_ + 3 * UCOPY_B) = ua;
    }
  }
  STAMP(1, 1);
  __syncthreads();
  STAMP(1, 2);
  if (role < 2) conv_main<0>(smem, ch, role & 1, lane);
  else conv_main<1>(smem, ch, role & 1, lane);
  STAMP(1, 3);
  __syncthreads();
  {
    const float* Y = (const float*)(smem + K2_Y2OFF) + c * 2048 + 8 * v;
    const f32x4 y0 = *(const f32x4*)Y + *(const f32x4*)(Y + 4096), y1 = *(const f32x4*)(Y + 4) + *(const f32x4*)(Y + 4100);
    u32x4 ho;
#pragma unroll
    for (int k = 0; k < 4; ++k) {
      const unsigned yb = k < 2 ? pack2(y0[2 * k], y0[2 * k + 1]) : pack2(y1[2 * k - 4], y1[2 * k - 3]);
      const unsigned sk = pack2(bflo(ua[k]) * dsk, bfhi(ua[k]) * dsk);
      const unsigned sm = pack2(bflo(yb) + bflo(sk), bfhi(yb) + bfhi(sk));
      ho[k] = pack2(bflo(xa[k]) + bflo(sm), bfhi(xa[k]) + bfhi(sm));
    }
    *(u32x4*)(ws + OFF_HT + (size_t)(v >> 3) * 65536 + (2 * blk + c) * 128 + (v & 7) * 16) = ho;
  }
  if (blk == 0 && t == 0) __hip_atomic_store((gu32*)(ws + OFF_EP), tag, RLX_AGENT);
  STAMP(1, 4);
}

constexpr int K3_HOFF = 0;
constexpr int K3_XOFF = 65536;
constexpr int K3_ROFF = 0;
constexpr int K3_SOFF = 131072;
constexpr int K3_COFF = K3_SOFF + 1024;
constexpr int K3_LDS = K3_COFF + 1536 + 6144;
#define PF_LOOP 1
#define PF_POST 2
#define W1_PFD 2
#define W1_EARLY 0

__device__ __forceinline__ int k3_swz(int row) { return ((row >> 1) & 1) | (((row >> 3) & 1) << 1); }
__device__ __forceinline__ float gelu_t(float xx) {
  const float c0 = -2.0f * 0.7978845608028654f * 1.4426950408889634f;
  const float u = xx * __builtin_fmaf(xx * xx, c0 * 0.044715f, c0);
  return xx * __builtin_amdgcn_rcpf(1.0f + __builtin_amdgcn_exp2f(u));
}
__device__ __forceinline__ u32x2 gelu4(f32x4 a, const float* cb, float m, float sc) {
  const f32x4 cs = *(const f32x4*)cb, bv = *(const f32x4*)(cb + 192);
  const float o0 = gelu_t(sc * (a[0] - m * cs[0]) + bv[0]), o1 = gelu_t(sc * (a[1] - m * cs[1]) + bv[1]);
  const float o2 = gelu_t(sc * (a[2] - m * cs[2]) + bv[2]), o3 = gelu_t(sc * (a[3] - m * cs[3]) + bv[3]);
  return (u32x2){pack2(o0, o1), pack2(o2, o3)};
}

__global__ __launch_bounds__(512, 2) void k34_mlp(const u16* __restrict__ b1, const u16* __restrict__ b2,
                                                  char* __restrict__ ws, u16* __restrict__ out) {
  __shared__ __attribute__((aligned(16))) char smem[K3_LDS];
  const int b = blockIdx.x, xcd = b & 7, idx = b >> 3;
  const int rt = 4 * xcd + (idx >> 3), fg = idx & 7;
  const int fstart = fg < 6 ? 22 * (fg >> 1) + 12 * (fg & 1) : 66 + 10 * (fg - 6);
  const int cnt = (fg < 6 && !(fg & 1)) ? 12 : 10;
  const int t = threadIdx.x, lane = t & 63;
  const int w = __builtin_amdgcn_readfirstlane(t >> 6);
  STAMP(2, 0);
  const int fq = w & 3, kh = w >> 2;
  const bool has3 = 8 + fq < cnt;
  const int perm = 2 * kh + (fq & 1);
  const int row0 = rt * 64;
  const int dq = fg;
  const u16* hT = (const u16*)(ws + OFF_HT);
  const int oct = t & 7, dr = t >> 3;
  const int g = lane >> 4, li = lane & 15, qq = li >> 2, p = lane & 3;
  float* SP = (float*)(smem + K3_SOFF);
  const int r0 = 8 * g + qq;
  const int sw0 = k3_swz(r0) * 32;
  const char* base0 = smem + K3_HOFF + r0 * 128 + p * 8;
  const int T0 = fstart + 2 * fq, T2 = fstart + (has3 ? 8 + fq : 0);
  f32x4 acc[3][4] = {};
  u32x4 hres;
  unsigned epoch;
  const int pg = w < 3 ? 2 * w : w == 3 ? 1 : w == 4 ? 3 : w;
  const int kb0 = pg < 6 ? 11 * (pg >> 1) + 6 * (pg & 1) : 33 + 5 * (pg - 6);
  const bool six = pg < 6 && !(pg & 1);
  const u32x4* Ap = (const u32x4*)(ws + OFF_W2) + ((size_t)(4 * dq) * KSF + kb0) * 64 + lane;
  u32x4 av[6][4];
  const int k5 = six ? 5 : 4;
  {
    u32x4 hv[8];
#pragma unroll
    for (int i = 0; i < 8; ++i) hv[i] = *(const u32x4*)(hT + (size_t)rt * 32768 + (t + 512 * i) * 8);
    const u32x4* W1a = (const u32x4*)(ws + OFF_W1) + (T0 * 16 + 8 * kh) * 64 + lane;
    const u32x4* W1c = (const u32x4*)(ws + OFF_W1) + (T2 * 16 + 8 * kh) * 64 + lane;
    f32x4 cpart;
    u32x2 bpart;
    const int cf = min(16 * fstart + 4 * (t & 63), F_ - 4);
    {
      const float* cp = (const float*)(ws + OFF_CS) + (2 * w) * F_ + cf;
      cpart = *(const f32x4*)cp + *(const f32x4*)(cp + F_);
      bpart = *(const u32x2*)(b1 + cf);
    }
    epoch = __hip_atomic_load((gu32*)(ws + OFF_EP), RLX_AGENT);
    u32x4 wv[3][8];
#pragma unroll
    for (int ks = 0; ks < W1_EARLY; ++ks) { wv[0][ks] = W1a[ks * 64]; wv[1][ks] = W1a[(16 + ks) * 64]; wv[2][ks] = W1c[ks * 64]; }
    SB();
    char* hdst = smem + K3_HOFF + dr * 128 + (((oct >> 1) ^ k3_swz(dr)) * 32) + (oct & 1) * 16;
#pragma unroll
    for (int i = 0; i < 8; ++i) *(u32x4*)(hdst + i * (64 * 128)) = hv[i];
    if (lane < 48) {
      *(f32x4*)(smem + K3_COFF + 1536 + w * 768 + lane * 16) = cpart;
      if (w == 0) *(f32x4*)(smem + K3_COFF + 768 + lane * 16) = (f32x4){bflo(bpart[0]), bfhi(bpart[0]), bflo(bpart[1]), bfhi(bpart[1])};
    }
    SB();
#pragma unroll
    for (int ks = W1_EARLY; ks < W1_PFD; ++ks) { wv[0][ks] = W1a[ks * 64]; wv[1][ks] = W1a[(16 + ks) * 64]; wv[2][ks] = W1c[ks * 64]; }
    SB();
    STAMP(2, 1);
    __syncthreads();
    STAMP(2, 2);
    if (w == 7 && lane < 48) {
      const f32x4* cp = (const f32x4*)(smem + K3_COFF + 1536) + lane;
      *(f32x4*)(smem + K3_COFF + lane * 16) = ((cp[0] + cp[48]) + (cp[96] + cp[144])) + ((cp[192] + cp[240]) + (cp[288] + cp[336]));
    }
    const u32x4 ones = {0x3f803f80u, 0x3f803f80u, 0x3f803f80u, 0x3f803f80u};
    f32x4 st0 = {}, st1 = {};
    const char* basek = base0 + kh * (8 * 4096);
    const char* basew = basek + ((fq * 32) ^ sw0);
    const int swp = sw0 ^ (perm * 32);
    bf16x8 B[2][4], fr[2];
    auto loadB = [&](int kk, int bf) {
#pragma unroll
      for (int lt = 0; lt < 4; ++lt) {
        const char* a = basek + kk * 4096 + ((lt * 32) ^ swp);
        const u32x2 v0 = trread(a), v1 = trread(a + 512);
        B[bf][lt] = asbf((u32x4){v0[0], v0[1], v1[0], v1[1]});
      }
      const u32x2 s0 = trread(basew + kk * 4096), s1 = trread(basew + kk * 4096 + 512);
      fr[bf] = asbf((u32x4){s0[0], s0[1], s1[0], s1[1]});
    };
    loadB(0, 0);
#pragma unroll
    for (int kk = 0; kk < 8; ++kk) {
      if (kk + W1_PFD < 8) {
        wv[0][kk + W1_PFD] = W1a[(kk + W1_PFD) * 64];
        wv[1][kk + W1_PFD] = W1a[(16 + kk + W1_PFD) * 64];
        wv[2][kk + W1_PFD] = W1c[(kk + W1_PFD) * 64];
      }
      if (kk >= 8 - PF_LOOP) {
#pragma unroll
        for (int i = 0; i < 4; ++i) av[kk - (8 - PF_LOOP)][i] = Ap[(i * KSF + kk - (8 - PF_LOOP)) * 64];
      }
      if (kk < 7) loadB(kk + 1, (kk + 1) & 1);
      SB();
      st0 = __builtin_amdgcn_mfma_f32_16x16x32_bf16(asbf(ones), fr[kk & 1], st0, 0, 0, 0);
      st1 = __builtin_amdgcn_mfma_f32_16x16x32_bf16(fr[kk & 1], fr[kk & 1], st1, 0, 0, 0);
#pragma unroll
      for (int n = 0; n < 3; ++n) {
#pragma unroll
        for (int lt = 0; lt < 4; ++lt)
          acc[n][lt] = __builtin_amdgcn_mfma_f32_16x16x32_bf16(asbf(wv[n][kk]), B[kk & 1][lt], acc[n][lt], 0, 0, 0);
      }
      SB();
    }
#pragma unroll
    for (int k = PF_LOOP; k < PF_POST; ++k)
#pragma unroll
      for (int i = 0; i < 4; ++i) av[k][i] = Ap[(i * KSF + k) * 64];
    SB();
    {
      const char* a = base0 + (2 * dq + (w & 1)) * 4096 + (((w >> 1) * 32) ^ sw0);
      const u32x2 v0 = trread(a), v1 = trread(a + 512);
      hres = (u32x4){v0[0], v0[1], v1[0], v1[1]};
    }
    if (g == qq) {
      SP[kh * 128 + 16 * fq + li] = st0[0];
      SP[kh * 128 + 64 + 16 * fq + li] = p == 0 ? st1[0] : p == 1 ? st1[1] : p == 2 ? st1[2] : st1[3];
    }
    f32x4* X = (f32x4*)(smem + K3_XOFF) + w * (8 * 64) + lane;
    const int o1 = (fq & 1) * 64;
    X[0 * 64 + o1] = acc[0][2]; X[1 * 64 - o1] = acc[0][3];
    X[2 * 64 + o1] = acc[1][2]; X[3 * 64 - o1] = acc[1][3];
#pragma unroll
    for (int lt = 1; lt < 4; ++lt) X[(4 + (lt ^ perm)) * 64] = acc[2][lt];
  }
  STAMP(2, 3);
  SB();
  __syncthreads();
  {
    const __amdgpu_buffer_rsrc_t rh = __builtin_amdgcn_make_buffer_rsrc(ws + OFF_HH, 0, L_ * F_ * 2, 0x00020000);
    const f32x4* XB = (const f32x4*)(smem + K3_XOFF) + lane;
    const f32x4* X = XB + (w ^ 4) * (8 * 64);
    const float* cb0 = (const float*)(smem + K3_COFF) + 32 * fq + 4 * g;
    float mm[2], ss[2];
#pragma unroll
    for (int h2 = 0; h2 < 2; ++h2) {
      const int l = 16 * (h2 ^ perm) + li;
      const float m = (SP[l] + SP[128 + l]) * (1.0f / D_);
      const float var = fmaxf((SP[64 + l] + SP[192 + l]) * (1.0f / D_) - m * m, 0.0f);
      const float r1 = __builtin_amdgcn_rsqf(var + 1e-5f);
      const float r2 = __builtin_amdgcn_rsqf(var * r1 * r1 + 1e-5f);
      mm[h2] = m; ss[h2] = r1 * r2;
    }
#pragma unroll
    for (int h2 = 0; h2 < 2; ++h2) {
      const int lt = h2 ^ perm, par = (h2 ^ fq) & 1;
      const u32x2 g0 = gelu4(acc[0][h2] + X[par * 64], cb0, mm[h2], ss[h2]);
      const u32x2 g1 = gelu4(acc[1][h2] + X[(2 + par) * 64], cb0 + 16, mm[h2], ss[h2]);
      __builtin_amdgcn_raw_buffer_store_b128((u32x4){g0[0], g0[1], g1[0], g1[1]}, rh,
                                             (((4 * rt + lt) * KSF + (T0 >> 1)) * 64 + lane) * 16, 0, 16);
      if (PF_POST + h2 < 6) {
#pragma unroll
        for (int i = 0; i < 4; ++i) av[PF_POST + h2][i] = Ap[(i * KSF + (PF_POST + h2 < 5 ? PF_POST + h2 : k5)) * 64];
      }
      SB();
    }
    if (has3) {
      const f32x4* Xo0 = XB + (w ^ 1) * (8 * 64);
      const f32x4* Xo1 = XB + (w ^ 5) * (8 * 64);
      const f32x4 aown = acc[2][0] + X[(4 + perm) * 64], aoth = Xo0[(4 + perm) * 64] + Xo1[(4 + perm) * 64];
      const bool odd = fq & 1;
      const float* cb2 = (const float*)(smem + K3_COFF) + 16 * (8 + (fq & 2)) + 4 * g;
      const u32x2 g0 = gelu4(odd ? aoth : aown, cb2, mm[0], ss[0]);
      const u32x2 g1 = gelu4(odd ? aown : aoth, cb2 + 16, mm[0], ss[0]);
      __builtin_amdgcn_raw_buffer_store_b128((u32x4){g0[0], g0[1], g1[0], g1[1]}, rh,
                                             (((4 * rt + perm) * KSF + (T2 >> 1)) * 64 + lane) * 16, 0, 16);
    }
  }
  STAMP(2, 4);
  asm volatile("s_waitcnt vmcnt(0)" ::: "memory");
  __builtin_amdgcn_s_barrier();
  gu64* flags = (gu64*)(ws + OFF_FL) + rt * (8 * 16);
  const unsigned tag = __builtin_amdgcn_readfirstlane(epoch);
  const unsigned long long fval = ((unsigned long long)tag << 32) | (unsigned)~tag;
  if (t == 0) __hip_atomic_store(flags + fg * 16, fval, RLX_AGENT);
  STAMP(2, 5);
#pragma unroll
  for (int k = PF_POST + 2; k < 6; ++k)
#pragma unroll
    for (int i = 0; i < 4; ++i) av[k][i] = Ap[(i * KSF + (k < 5 ? k : k5)) * 64];
  const u32x4 bias = *(const u32x4*)(b2 + 64 * dq + 32 * (w & 1) + 8 * g);
  SB();
  for (unsigned spins = 0; spins < (1u << 22); ++spins) {
    if (__all(__hip_atomic_load(flags + pg * 16, RLX_AGENT) == fval)) break;
    __builtin_amdgcn_s_sleep(1);
  }
  __builtin_amdgcn_fence(__ATOMIC_ACQUIRE, "wavefront");
  STAMP(2, 6);
  f32x4 oc[4][4] = {};
  {
    const __amdgpu_buffer_rsrc_t rs = __builtin_amdgcn_make_buffer_rsrc(ws + OFF_HH, 0, L_ * F_ * 2, 0x00020000);
    const int boff = (((4 * rt) * KSF + kb0) * 64 + lane) * 16;
    constexpr int NB = 3;
    u32x4 bv[NB][4];
#pragma unroll
    for (int k = 0; k < NB; ++k)
#pragma unroll
      for (int i = 0; i < 4; ++i) bv[k][i] = __builtin_amdgcn_raw_buffer_load_b128(rs, boff + (i * KSF + k) * 1024, 0, 16);
    SB();
#pragma unroll
    for (int k = 0; k < 6; ++k) {
      if (k < 5 || six) {
#pragma unroll
        for (int a = 0; a < 4; ++a)
#pragma unroll
          for (int bb = 0; bb < 4; ++bb)
            oc[a][bb] = __builtin_amdgcn_mfma_f32_16x16x32_bf16(asbf(av[k][a]), asbf(bv[k % NB][bb]), oc[a][bb], 0, 0, 0);
      }
      if (k + NB < 6) {
        const int kn = k + NB < 5 ? k + NB : k5;
#pragma unroll
        for (int i = 0; i < 4; ++i)
          bv[k % NB][i] = __builtin_amdgcn_raw_buffer_load_b128(rs, boff + (i * KSF + kn) * 1024, 0, 16);
      }
      SB();
    }
  }
  STAMP(2, 7);
  f32x4* R = (f32x4*)(smem + K3_ROFF);
#pragma unroll
  for (int bb = 0; bb < 4; ++bb)
#pragma unroll
    for (int a = 0; a < 4; ++a) R[((w * 4 + bb) * 4 + a) * 64 + lane] = oc[a][bb];
  __syncthreads();
  f32x4 sum[2];
#pragma unroll
  for (int a = 0; a < 2; ++a) {
    const f32x4* Rp = R + (((w >> 1)) * 4 + 2 * (w & 1) + a) * 64 + lane;
    f32x4 s0 = Rp[0 * 1024] + Rp[1 * 1024], s1 = Rp[2 * 1024] + Rp[3 * 1024];
    f32x4 s2 = Rp[4 * 1024] + Rp[5 * 1024], s3 = Rp[6 * 1024] + Rp[7 * 1024];
    sum[a] = (s0 + s1) + (s2 + s3);
  }
  const int lrow = row0 + 16 * (w >> 1) + li;
  u32x4 o;
#pragma unroll
  for (int hf = 0; hf < 2; ++hf) {
#pragma unroll
    for (int k = 0; k < 2; ++k) {
      const unsigned hb = hres[2 * hf + k], bb = bias[2 * hf + k];
      o[2 * hf + k] = pack2(bflo(hb) + (sum[hf][2 * k] + bflo(bb)), bfhi(hb) + (sum[hf][2 * k + 1] + bfhi(bb)));
    }
  }
  *(u32x4*)(out + (size_t)lrow * D_ + 64 * dq + 32 * (w & 1) + 8 * g) = o;
  STAMP(2, 8);
}

extern "C" void kernel_launch(void* const* d_in, const int* in_sizes, int n_in, void* d_out, int out_size, void* d_ws,
                              size_t ws_size, hipStream_t stream) {
  const u16* x = (const u16*)d_in[0];
  const u16* la = (const u16*)d_in[1];
  const u16* ai = (const u16*)d_in[2];
  const u16* cr = (const u16*)d_in[3];
  const u16* ci = (const u16*)d_in[4];
  const u16* ldt = (const u16*)d_in[5];
  const u16* dsk = (const u16*)d_in[6];
  const u16* w1 = (const u16*)d_in[8];
  const u16* b1 = (const u16*)d_in[9];
  const u16* w2 = (const u16*)d_in[10];
  const u16* b2 = (const u16*)d_in[11];
  char* ws = (char*)d_ws;
  k2_conv<<<256, 512, 0, stream>>>(x, la, ai, cr, ci, ldt, dsk, w1, w2, ws);
  k34_mlp<<<256, 512, 0, stream>>>(b1, b2, ws, (u16*)d_out);
}
```

```cpp
#include <hip/hip_runtime.h>
#include <hip/hip_bf16.h>
#include <stdint.h>

typedef __attribute__((ext_vector_type(8))) __bf16 bf16x8;
typedef __attribute__((ext_vector_type(2))) __bf16 bf16x2;
typedef __attribute__((ext_vector_type(8))) _Float16 f16x8;
typedef __attribute__((ext_vector_type(2))) _Float16 f16x2;
typedef __attribute__((ext_vector_type(4))) float f32x4;
typedef __attribute__((ext_vector_type(2))) float f32x2;
typedef __attribute__((ext_vector_type(16))) float f32x16;
typedef __attribute__((ext_vector_type(4))) unsigned u32x4;
typedef __attribute__((ext_vector_type(2))) unsigned u32x2;
typedef __attribute__((ext_vector_type(4))) short s16x4;
typedef unsigned short u16;

#define LDS_AS __attribute__((address_space(3)))
#define SB() __builtin_amdgcn_sched_barrier(0)
constexpr int NREP = 8;
constexpr int POLL_SLEEP = 4;

constexpr int L_ = 2048, D_ = 512, NS_ = 128, F_ = 1376;
constexpr int KSF = F_ / 32;

constexpr size_t OFF_EP = 0;
constexpr size_t OFF_FL = 128u << 10;
constexpr size_t OFF_CS = 256u << 10;
constexpr size_t OFF_F1 = 512u << 10;
constexpr size_t OFF_HT = 4u << 20;
constexpr size_t OFF_UX = 8u << 20;
constexpr size_t OFF_W1 = 13u << 20;
constexpr size_t OFF_W2 = 15u << 20;
constexpr size_t OFF_HH = 18u << 20;
typedef __attribute__((address_space(1))) unsigned gu32;
typedef __attribute__((address_space(1))) unsigned long long gu64;
#define RLX_AGENT __ATOMIC_RELAXED, __HIP_MEMORY_SCOPE_AGENT

constexpr int KIMG = 2112;
constexpr int KIMG_B = KIMG * 2;

__device__ __forceinline__ unsigned pack2(float a, float b) {
  f32x2 v = {a, b};
  bf16x2 r = __builtin_convertvector(v, bf16x2);
  return __builtin_bit_cast(unsigned, r);
}
__device__ __forceinline__ unsigned pack2h(float a, float b) {
  f32x2 v = {a, b};
  f16x2 r = __builtin_convertvector(v, f16x2);
  return __builtin_bit_cast(unsigned, r);
}
__device__ __forceinline__ float bflo(unsigned u) { return __builtin_bit_cast(float, u << 16); }
__device__ __forceinline__ float bfhi(unsigned u) { return __builtin_bit_cast(float, u & 0xffff0000u); }
__device__ __forceinline__ float bf2f(u16 u) { return __builtin_bit_cast(float, (unsigned)u << 16); }
__device__ __forceinline__ float bfround(float a) { return bflo(pack2(a, a)); }
__device__ __forceinline__ bf16x8 asbf(u32x4 v) { return __builtin_bit_cast(bf16x8, v); }
__device__ __forceinline__ f16x8 ash(u32x4 v) { return __builtin_bit_cast(f16x8, v); }
__device__ __forceinline__ u32x2 trread(const void* p) {
  s16x4 v = __builtin_amdgcn_ds_read_tr16_b64_v4i16((LDS_AS s16x4*)(p));
  return __builtin_bit_cast(u32x2, v);
}
template <int CTRL, int RMASK>
__device__ __forceinline__ float dppf(float idv, float v) {
  return __builtin_bit_cast(float, __builtin_amdgcn_update_dpp(__builtin_bit_cast(int, idv), __builtin_bit_cast(int, v),
                                                               CTRL, RMASK, 0xf, false));
}
__device__ __forceinline__ float wave_sum(float v) {
  v += dppf<0x111, 0xf>(0.f, v);
  v += dppf<0x112, 0xf>(0.f, v);
  v += dppf<0x114, 0xf>(0.f, v);
  v += dppf<0x118, 0xf>(0.f, v);
  v += dppf<0x142, 0xa>(0.f, v);
  v += dppf<0x143, 0xc>(0.f, v);
  return __builtin_bit_cast(float, __builtin_amdgcn_readlane(__builtin_bit_cast(int, v), 63));
}
__device__ __forceinline__ float wave_max(float v) {
  const float ni = -__builtin_inff();
  v = fmaxf(v, dppf<0x111, 0xf>(ni, v));
  v = fmaxf(v, dppf<0x112, 0xf>(ni, v));
  v = fmaxf(v, dppf<0x114, 0xf>(ni, v));
  v = fmaxf(v, dppf<0x118, 0xf>(ni, v));
  v = fmaxf(v, dppf<0x142, 0xa>(ni, v));
  v = fmaxf(v, dppf<0x143, 0xc>(ni, v));
  return __builtin_bit_cast(float, __builtin_amdgcn_readlane(__builtin_bit_cast(int, v), 63));
}

constexpr int UCOPY_B = 4640;
constexpr int K2_KOFF = 0;
constexpr int K2_UOFF = 2 * 2 * KIMG_B;
constexpr int K2_UCH = 4 * UCOPY_B;
constexpr int K2_YOFF = K2_UOFF + 2 * K2_UCH;
constexpr int K2_MOFF = K2_YOFF + 2 * 2048 * 4;
constexpr int K2_TOFF = K2_MOFF + 64;
constexpr int K2_FOFF = K2_TOFF + 8 * 24 * 32 * 4;
constexpr int FSTR = 128 * 4 + 16;
constexpr int F_B1 = 0, F_B2 = 4, F_A1 = 12, F_A2 = 20, F_N = 29;
constexpr int K2_Y2OFF = K2_FOFF;
constexpr int K2_LDS = K2_FOFF + 32768;

__device__ __forceinline__ f32x2 cmul(f32x2 a, f32x2 b) { return (f32x2){a.x * b.x - a.y * b.y, a.x * b.y + a.y * b.x}; }
__device__ __forceinline__ unsigned cmulh(unsigned a, unsigned b) {
  unsigned t, r;
  asm("v_pk_mul_f16 %0, %1, %2 op_sel:[0,0] op_sel_hi:[1,0]" : "=v"(t) : "v"(a), "v"(b));
  asm("v_pk_fma_f16 %0, %1, %2, %3 op_sel:[1,1,0] op_sel_hi:[0,1,1] neg_lo:[1,0,0]" : "=v"(r) : "v"(a), "v"(b), "v"(t));
  return r;
}
__device__ __forceinline__ f32x2 cexp2r(float x, float y) {
  const float e = __builtin_amdgcn_exp2f(x), th = __builtin_amdgcn_fractf(y);
  return (f32x2){e * __builtin_amdgcn_cosf(th), e * __builtin_amdgcn_sinf(th)};
}

struct TOff { u16 v[2064]; };
constexpr TOff make_toff() {
  TOff t{};
  for (int q = 0; q < 2064; ++q) {
    const int lag = q <= 2047 ? 2047 - q : 0;
    const int s = lag >= 1024 ? 3 : lag >= 512 ? 2 : lag >= 256 ? 1 : 0;
    const int pp = s ? ((lag + (1 << (s - 1)) - 1 + ((lag >> s) & 1)) >> s) << s : lag;
    const int r2 = pp >> 8, rp = pp & 7, col = (pp & 255) >> 3;
    const int row = r2 == 0 ? rp : r2 == 1 ? 8 + (rp >> 1) : r2 < 4 ? 8 + 2 * r2 + (rp >> 2) : 12 + r2;
    t.v[q] = (u16)((row * 32 + col) * 4);
  }
  return t;
}
__device__ __attribute__((aligned(16))) const TOff g_toff = make_toff();

template <int TS>
__device__ __forceinline__ void conv_main(char* smem, int ch, int rh, int lane) {
  constexpr int NF = TS == 0 ? 8 : 6;
  const int j = lane & 15, gq = lane >> 4;
  const char* abase = smem + K2_KOFF + ch * (2 * KIMG_B) + ((j & 1) ? 0 : KIMG_B) + 2 * (2072 - 8 * (j >> 1) + 8 * gq) -
                      64 * (4 * rh + 3) - 512 * 7;
  const char* bbase = smem + K2_UOFF + ch * K2_UCH + (j & 3) * UCOPY_B + 2 * (232 + 64 * (j >> 2) + 8 * gq) -
                      64 * (4 * rh + 3);
  f32x4 acc[8] = {};
  bf16x8 A[2][NF], B[2][NF];
#pragma unroll
  for (int a = 0; a < NF; ++a) {
    A[0][a] = *(const bf16x8*)(abase + 512 * (7 - a) + 64 * 3);
    B[0][a] = *(const bf16x8*)(bbase + 512 * a + 64 * 3);
  }
#pragma unroll
  for (int rr = 0; rr < 4; ++rr) {
    if (rr < 3) {
#pragma unroll
      for (int a = 0; a < NF; ++a) {
        A[(rr + 1) & 1][a] = *(const bf16x8*)(abase + 512 * (7 - a) + 64 * (2 - rr));
        B[(rr + 1) & 1][a] = *(const bf16x8*)(bbase + 512 * a + 64 * (2 - rr));
      }
    }
    SB();
#pragma unroll
    for (int a = 0; a < NF; ++a)
#pragma unroll
      for (int b = 0; b < NF; ++b) {
        const int nt = a + b;
        const bool use = TS == 0 ? (nt == 7 || nt == 6 || nt == 2) : (nt <= 5 && nt != 2);
        if (use) acc[nt] = __builtin_amdgcn_mfma_f32_16x16x32_bf16(A[rr & 1][a], B[rr & 1][b], acc[nt], 0, 0, 0);
      }
    SB();
  }
  float* Y = (float*)(smem + K2_Y2OFF) + (rh * 2 + ch) * 2048 + 16 * gq + 2 * (j & 3) + 64 * (j >> 2);
#pragma unroll
  for (int nt = 0; nt < 8; ++nt) {
    const bool use = TS == 0 ? (nt == 7 || nt == 6 || nt == 2) : (nt <= 5 && nt != 2);
    if (use) {
      *(f32x2*)(Y + 256 * nt) = (f32x2){acc[nt][0], acc[nt][1]};
      *(f32x2*)(Y + 256 * nt + 8) = (f32x2){acc[nt][2], acc[nt][3]};
    }
  }
}

__global__ __launch_bounds__(512, 2) void k2_conv(const u16* __restrict__ x, const u16* __restrict__ la,
                                               const u16* __restrict__ ai, const u16* __restrict__ cr,
                                               const u16* __restrict__ ci, const u16* __restrict__ ldt,
                                               const u16* __restrict__ dskip, const u16* __restrict__ w1,
                                               const u16* __restrict__ w2, char* __restrict__ ws) {
  __shared__ __attribute__((aligned(16))) char smem[K2_LDS];
  const int t = threadIdx.x, lane = t & 63;
  const int w = __builtin_amdgcn_readfirstlane(t >> 6);
  const int blk = blockIdx.x;
  const int ch = w >> 2;
  const int d = 2 * blk + ch;
  float* SC = (float*)(smem + K2_MOFF);
  float* T = (float*)(smem + K2_TOFF);
  u32x4 xrow[2];
#pragma unroll
  for (int i = 0; i < 2; ++i) xrow[i] = *(const u32x4*)(x + (size_t)(16 * (blk >> 1) + 2 * w + i) * D_ + lane * 8);
  const unsigned epoch = __hip_atomic_load((gu32*)(ws + OFF_EP), RLX_AGENT);
  const int half = (w >> 1) & 1, n = t & 127;
  const int pi = d * NS_ + n;
  const u16 p_la = la[pi], p_ai = ai[pi], p_dt = ldt[d];
  u16 p_cr = 0, p_ci = 0, q_cr = 0, q_ci = 0;
  if (half == 0) { p_cr = cr[pi]; p_ci = ci[pi]; q_cr = cr[pi ^ 64]; q_ci = ci[pi ^ 64]; }
  const int c = t >> 8, v = t & 255;
  const float dsk = bf2f(dskip[2 * blk + (t >> 8)]);
  const u32x4 tof = *(const u32x4*)(g_toff.v + 8 * v);
  const unsigned tof8 = g_toff.v[8 * v + 8];
  const int role = w & 3;
  SB();
  const int g4 = lane >> 4, qq = (lane & 15) >> 2, p4 = lane & 3;
  {
    float s1[2], s2[2];
#pragma unroll
    for (int i = 0; i < 2; ++i) {
      s1[i] = 0.f; s2[i] = 0.f;
#pragma unroll
      for (int j = 0; j < 4; ++j) {
        const float a = bflo(xrow[i][j]), b = bfhi(xrow[i][j]);
        s1[i] += a + b;
        s2[i] += a * a + b * b;
      }
    }
#pragma unroll
    for (int i = 0; i < 2; ++i) { s1[i] = wave_sum(s1[i]) * (1.0f / D_); s2[i] = wave_sum(s2[i]) * (1.0f / D_); }
    char* S = smem + K2_TOFF;
    if ((lane >> 5) == (blk & 1)) {
#pragma unroll
      for (int i = 0; i < 2; ++i) {
        const float rs = __builtin_amdgcn_rsqf(fmaxf(s2[i] - s1[i] * s1[i], 0.0f) + 1e-5f);
        u32x4 uv;
#pragma unroll
        for (int j = 0; j < 4; ++j) uv[j] = pack2((bflo(xrow[i][j]) - s1[i]) * rs, (bfhi(xrow[i][j]) - s1[i]) * rs);
        *(u32x4*)(S + (2 * w + i) * 544 + (lane & 31) * 16) = uv;
        *(u32x4*)(S + 8704 + (2 * w + i) * 544 + (lane & 31) * 16) = xrow[i];
      }
    }
    __syncthreads();
    const __amdgpu_buffer_rsrc_t rux = __builtin_amdgcn_make_buffer_rsrc(ws + OFF_UX, 0, D_ * 8192, 0x00020000);
    const char* a = S + (g4 >> 1) * 8704 + (8 * (g4 & 1) + qq) * 544 + (32 * w + 4 * p4) * 2;
#pragma unroll
    for (int r = 0; r < 2; ++r) {
      const u32x2 v0 = trread(a + r * 32), v1 = trread(a + r * 32 + 4 * 544);
      const int dd = 256 * (blk & 1) + 32 * w + 16 * r + (lane & 15);
      __builtin_amdgcn_raw_buffer_store_b128((u32x4){v0[0], v0[1], v1[0], v1[1]}, rux, dd * 8192 + (blk >> 1) * 64 + g4 * 16, 0, 16);
    }
  }
  {
    const float dt = bfround(__builtin_amdgcn_exp2f(bf2f(p_dt) * 1.4426950408889634f));
    const float are = -bfround(__builtin_amdgcn_exp2f(bf2f(p_la) * 1.4426950408889634f));
    const float ax = (are * dt) * 1.4426950408889634f, ay = (bf2f(p_ai) * dt) * 0.15915494309189535f;
    char* F = smem + K2_FOFF + ch * (F_N * FSTR) + n * 4;
    auto put = [&](int e, f32x2 z) { *(unsigned*)(F + e * FSTR) = pack2h(z.x, z.y); };
    if (half == 0) {
      const float mx = wave_max(fmaxf(fmaxf(fabsf(bf2f(p_cr)), fabsf(bf2f(p_ci))), fmaxf(fabsf(bf2f(q_cr)), fabsf(bf2f(q_ci)))));
      const unsigned eb = min((__builtin_bit_cast(unsigned, mx) >> 23) + 2u, 252u);
      const float inv = __builtin_bit_cast(float, (254u - eb) << 23);
      if (n == 0) SC[8 + ch] = 2.0f * dt * __builtin_bit_cast(float, eb << 23);
      const f32x2 z1 = cexp2r(ax, -ay), z8 = cexp2r(8.0f * ax, 8.0f * ay);
      const f32x2 z16 = cmul(z8, z8);
      put(F_B1 + 0, (f32x2){1.0f, 0.0f});
      put(F_B1 + 1, z8);
      put(F_B1 + 2, z16);
      put(F_B1 + 3, cmul(z16, z8));
      f32x2 a1[8];
      a1[0] = (f32x2){bf2f(p_cr) * inv, -bf2f(p_ci) * inv};
      const f32x2 z2 = cmul(z1, z1), z4 = cmul(z2, z2);
      a1[1] = cmul(a1[0], z1); a1[2] = cmul(a1[0], z2); a1[3] = cmul(a1[1], z2);
#pragma unroll
      for (int k = 0; k < 4; ++k) a1[4 + k] = cmul(a1[k], z4);
#pragma unroll
      for (int k = 0; k < 8; ++k) put(F_A1 + k, a1[k]);
    } else {
      const f32x2 y1 = cexp2r(32.0f * ax, 32.0f * ay), x1 = cexp2r(256.0f * ax, -256.0f * ay);
      const f32x2 y2 = cmul(y1, y1), y4 = cmul(y2, y2), y3 = cmul(y2, y1);
      put(F_B2 + 0, (f32x2){1.0f, 0.0f});
      put(F_B2 + 1, y1); put(F_B2 + 2, y2); put(F_B2 + 3, y3); put(F_B2 + 4, y4);
      put(F_B2 + 5, cmul(y4, y1)); put(F_B2 + 6, cmul(y4, y2)); put(F_B2 + 7, cmul(y4, y3));
      const f32x2 x2 = cmul(x1, x1), x4 = cmul(x2, x2), x3 = cmul(x2, x1);
      put(F_A2 + 0, (f32x2){1.0f, 0.0f});
      put(F_A2 + 1, x1); put(F_A2 + 2, x2); put(F_A2 + 3, x3); put(F_A2 + 4, x4);
      put(F_A2 + 5, cmul(x4, x1)); put(F_A2 + 6, cmul(x4, x2)); put(F_A2 + 7, cmul(x4, x3)); put(F_A2 + 8, cmul(x4, x4));
      const int z = (ch << 7) | n;
      if (z < 200) {
        const int cc = z / 100, rem = z - 100 * cc, cix = rem / 25, m = rem - 25 * cix;
        *(u32x4*)(smem + K2_UOFF + cc * K2_UCH + cix * UCOPY_B + m * 16) = (u32x4){0u, 0u, 0u, 0u};
      } else if (z < 232) {
        const int i = z - 200;
        *(u32x4*)(smem + K2_KOFF + (i >> 3) * KIMG_B + (256 + (i & 7)) * 16) = (u32x4){0u, 0u, 0u, 0u};
      }
    }
  }
  asm volatile("s_waitcnt vmcnt(0)" ::: "memory");
  __syncthreads();
  const unsigned tag = __builtin_amdgcn_readfirstlane(epoch) + 1u;
  const unsigned long long fval = ((unsigned long long)tag << 32) | (unsigned)~tag;
  if (t < NREP) __hip_atomic_store((gu64*)(ws + OFF_F1) + t * 512 + blk, fval, RLX_AGENT);
  const int gw = (w >= 1 && w <= 6) ? blk + 256 * (w - 1) : 1376;
  u32x4 wl[2];
  if (gw < 1376) {
    const u16* src;
    int rs;
    if (gw < 688) { src = w1 + (size_t)(32 * (gw & 15)) * F_ + 32 * (gw >> 4); rs = F_; }
    else { const int u2 = gw - 688, ap = u2 / KSF, kb = u2 - ap * KSF; src = w2 + (size_t)(32 * kb) * D_ + 32 * ap; rs = D_; }
    src += (lane >> 2) * rs + (lane & 3) * 8;
    wl[0] = *(const u32x4*)src;
    wl[1] = *(const u32x4*)(src + 16 * rs);
  }
  SB();
  {
    const int kq = w & 3;
    const int r = lane & 31, hh = lane >> 5;
    const int r1 = r < 8 ? r : r < 12 ? 2 * (r - 8) : r < 16 ? 4 * (r & 1) : 0;
    const int r2 = r < 8 ? 0 : r < 12 ? 1 : r < 16 ? 2 + ((r - 12) >> 1) : r < 21 ? r - 12 : 0;
    const char* F = smem + K2_FOFF + ch * (F_N * FSTR) + (kq * 32 + 4 * hh) * 4;
    const char* fb1 = F + (F_B1 + (r & 3)) * FSTR;
    const char* fb2 = F + (F_B2 + (r >> 2)) * FSTR;
    const char* fa1 = F + (F_A1 + r1) * FSTR;
    const char* fa2 = F + (F_A2 + r2) * FSTR;
    f32x16 acc = {};
#pragma unroll
    for (int ks = 0; ks < 4; ++ks) {
      const u32x4 b1v = *(const u32x4*)(fb1 + ks * 32), b2v = *(const u32x4*)(fb2 + ks * 32);
      const u32x4 a1v = *(const u32x4*)(fa1 + ks * 32), a2v = *(const u32x4*)(fa2 + ks * 32);
      u32x4 av, bv;
#pragma unroll
      for (int q = 0; q < 4; ++q) {
        av[q] = cmulh(a1v[q], a2v[q]);
        bv[q] = cmulh(b1v[q], b2v[q]);
      }
      acc = __builtin_amdgcn_mfma_f32_32x32x16_f16(ash(av), ash(bv), acc, 0, 0, 0);
    }
    float* Tw = T + w * (24 * 32);
#pragma unroll
    for (int g = 0; g < 12; ++g) Tw[((g & 3) + 8 * (g >> 2) + 4 * hh) * 32 + r] = acc[g];
  }
  if (w == 0) {
    const gu64* fp = (const gu64*)(ws + OFF_F1) + (blk % NREP) * 512 + lane;
    unsigned long long fa[3][4];
#pragma unroll
    for (int k = 0; k < 2; ++k) {
#pragma unroll
      for (int i = 0; i < 4; ++i) fa[k][i] = __hip_atomic_load(fp + 64 * i, RLX_AGENT);
      __builtin_amdgcn_s_sleep(POLL_SLEEP);
    }
    bool done = false;
    for (unsigned spins = 0; spins < (1u << 19) && !done; ++spins) {
#pragma unroll
      for (int k = 0; k < 3; ++k) {
        if (!done) {
#pragma unroll
          for (int i = 0; i < 4; ++i) fa[(k + 2) % 3][i] = __hip_atomic_load(fp + 64 * i, RLX_AGENT);
          done = __all((fa[k][0] == fval) & (fa[k][1] == fval) & (fa[k][2] == fval) & (fa[k][3] == fval));
        }
      }
    }
  }
  __syncthreads();
  u32x4 ua, ub, xa;
  {
    const __amdgpu_buffer_rsrc_t rux = __builtin_amdgcn_make_buffer_rsrc(ws + OFF_UX + (size_t)blk * 16384, 0, 16384, 0x00020000);
    const int o = (t >> 1) * 64 + (t & 1) * 16;
    ua = __builtin_amdgcn_raw_buffer_load_b128(rux, o, 0, 16);
    xa = __builtin_amdgcn_raw_buffer_load_b128(rux, o + 32, 0, 16);
    ub = __builtin_amdgcn_raw_buffer_load_b128(rux, v < 255 ? o + ((t & 1) ? 48 : 16) : 1 << 20, 0, 16);
  }
  SB();
  {
    const char* Tq = smem + K2_TOFF + c * 4 * (24 * 32 * 4);
    const float sc = SC[8 + c];
    float kv[9];
#pragma unroll
    for (int jj = 0; jj < 9; ++jj) {
      const char* a = Tq + (jj == 8 ? tof8 : (jj & 1) ? (tof[jj >> 1] >> 16) : (tof[jj >> 1] & 0xffffu));
      kv[jj] = ((*(const float*)a + *(const float*)(a + 3072)) + (*(const float*)(a + 6144) + *(const float*)(a + 9216))) * sc;
    }
    if (v == 255) kv[8] = 0.0f;
    u32x4 c0, c1;
#pragma unroll
    for (int jj = 0; jj < 4; ++jj) {
      c0[jj] = pack2(kv[2 * jj], kv[2 * jj + 1]);
      c1[jj] = pack2(kv[2 * jj + 1], kv[2 * jj + 2]);
    }
    char* dst = smem + K2_KOFF + c * (2 * KIMG_B) + v * 16;
    *(u32x4*)dst = c0;
    *(u32x4*)(dst + KIMG_B) = c1;
  }
  if (gw < 1376) {
    char* S = smem + K2_YOFF + w * 2048;
    *(u32x4*)(S + lane * 16) = wl[0];
    *(u32x4*)(S + 1024 + lane * 16) = wl[1];
    u32x4 o[2];
    int dst, dstr;
    if (gw < 688) {
#pragma unroll
      for (int f = 0; f < 2; ++f) {
        const char* a = S + (8 * g4 + qq) * 64 + f * 32 + p4 * 8;
        const u32x2 v0 = trread(a), v1 = trread(a + 4 * 64);
        o[f] = (u32x4){v0[0], v0[1], v1[0], v1[1]};
      }
      dst = ((2 * (gw >> 4)) * 16 + (gw & 15)) * 1024;
      dstr = 16 * 1024;
      float c2[2];
#pragma unroll
      for (int f = 0; f < 2; ++f) {
        c2[f] = ((bflo(o[f][0]) + bfhi(o[f][0])) + (bflo(o[f][1]) + bfhi(o[f][1]))) +
                ((bflo(o[f][2]) + bfhi(o[f][2])) + (bflo(o[f][3]) + bfhi(o[f][3])));
        c2[f] += __shfl_xor(c2[f], 16);
        c2[f] += __shfl_xor(c2[f], 32);
      }
      if (lane < 32) ((float*)(ws + OFF_CS))[(gw & 15) * F_ + 32 * (gw >> 4) + lane] = lane < 16 ? c2[0] : c2[1];
    } else {
#pragma unroll
      for (int f = 0; f < 2; ++f) {
        const char* a = S + (4 * g4 + qq) * 64 + p4 * 16 + f * 8;
        const u32x2 v0 = trread(a), v1 = trread(a + 16 * 64);
        o[f] = (u32x4){v0[0], v0[1], v1[0], v1[1]};
      }
      const int u2 = gw - 688, ap = u2 / KSF, kb = u2 - ap * KSF;
      dst = (int)(OFF_W2 - OFF_W1) + ((2 * ap) * KSF + kb) * 1024;
      dstr = KSF * 1024;
    }
    const __amdgpu_buffer_rsrc_t rw = __builtin_amdgcn_make_buffer_rsrc(ws + OFF_W1, 0, 4 << 20, 0x00020000);
    __builtin_amdgcn_raw_buffer_store_b128(o[0], rw, dst + lane * 16, 0, 0);
    __builtin_amdgcn_raw_buffer_store_b128(o[1], rw, dst + dstr + lane * 16, 0, 0);
  }
  SB();
  {
    char* ub_ = smem + K2_UOFF + c * K2_UCH + (v + 26) * 16;
    *(u32x4*)(ub_ + 0 * UCOPY_B) = (u32x4){ua[1], ua[2], ua[3], ub[0]};
    *(u32x4*)(ub_ + 1 * UCOPY_B) = (u32x4){ua[2], ua[3], ub[0], ub[1]};
    *(u32x4*)(ub_ + 2 * UCOPY_B) = (u32x4){ua[3], ub[0], ub[1], ub[2]};
    *(u32x4*)(ub_ + 3 * UCOPY_B) = ub;
    if (v == 0) {
      ub_ -= 16;
      *(u32x4*)(ub_ + 0 * UCOPY_B) = (u32x4){0u, 0u, 0u, ua[0]};
      *(u32x4*)(ub_ + 1 * UCOPY_B) = (u32x4){0u, 0u, ua[0], ua[1]};
      *(u32x4*)(ub_ + 2 * UCOPY_B) = (u32x4){0u, ua[0], ua[1], ua[2]};
      *(u32x4*)(ub_ + 3 * UCOPY_B) = ua;
    }
  }
  __syncthreads();
  if (role < 2) conv_main<0>(smem, ch, role & 1, lane);
  else conv_main<1>(smem, ch, role & 1, lane);
  __syncthreads();
  {
    const float* Y = (const float*)(smem + K2_Y2OFF) + c * 2048 + 8 * v;
    const f32x4 y0 = *(const f32x4*)Y + *(const f32x4*)(Y + 4096), y1 = *(const f32x4*)(Y + 4) + *(const f32x4*)(Y + 4100);
    u32x4 ho;
#pragma unroll
    for (int k = 0; k < 4; ++k) {
      const unsigned yb = k < 2 ? pack2(y0[2 * k], y0[2 * k + 1]) : pack2(y1[2 * k - 4], y1[2 * k - 3]);
      const unsigned sk = pack2(bflo(ua[k]) * dsk, bfhi(ua[k]) * dsk);
      const unsigned sm = pack2(bflo(yb) + bflo(sk), bfhi(yb) + bfhi(sk));
      ho[k] = pack2(bflo(xa[k]) + bflo(sm), bfhi(xa[k]) + bfhi(sm));
    }
    *(u32x4*)(ws + OFF_HT + (size_t)(v >> 3) * 65536 + (2 * blk + c) * 128 + (v & 7) * 16) = ho;
  }
  if (blk == 0 && t == 0) __hip_atomic_store((gu32*)(ws + OFF_EP), tag, RLX_AGENT);
}

constexpr int K3_HOFF = 0;
constexpr int K3_XOFF = 65536;
constexpr int K3_ROFF = 0;
constexpr int K3_SOFF = 131072;
constexpr int K3_COFF = K3_SOFF + 1024;
constexpr int K3_LDS = K3_COFF + 1536 + 6144;
constexpr int PF_LOOP = 1, PF_POST = 2;
constexpr int W1_PFD = 2;

__device__ __forceinline__ int k3_swz(int row) { return ((row >> 1) & 1) | (((row >> 3) & 1) << 1); }
__device__ __forceinline__ float gelu_t(float xx) {
  const float c0 = -2.0f * 0.7978845608028654f * 1.4426950408889634f;
  const float u = xx * __builtin_fmaf(xx * xx, c0 * 0.044715f, c0);
  return xx * __builtin_amdgcn_rcpf(1.0f + __builtin_amdgcn_exp2f(u));
}
__device__ __forceinline__ u32x2 gelu4(f32x4 a, const float* cb, float m, float sc) {
  const f32x4 cs = *(const f32x4*)cb, bv = *(const f32x4*)(cb + 192);
  const float o0 = gelu_t(sc * (a[0] - m * cs[0]) + bv[0]), o1 = gelu_t(sc * (a[1] - m * cs[1]) + bv[1]);
  const float o2 = gelu_t(sc * (a[2] - m * cs[2]) + bv[2]), o3 = gelu_t(sc * (a[3] - m * cs[3]) + bv[3]);
  return (u32x2){pack2(o0, o1), pack2(o2, o3)};
}

__global__ __launch_bounds__(512, 2) void k34_mlp(const u16* __restrict__ b1, const u16* __restrict__ b2,
                                                  char* __restrict__ ws, u16* __restrict__ out) {
  __shared__ __attribute__((aligned(16))) char smem[K3_LDS];
  const int b = blockIdx.x, xcd = b & 7, idx = b >> 3;
  const int rt = 4 * xcd + (idx >> 3), fg = idx & 7;
  const int fstart = fg < 6 ? 22 * (fg >> 1) + 12 * (fg & 1) : 66 + 10 * (fg - 6);
  const int cnt = (fg < 6 && !(fg & 1)) ? 12 : 10;
  const int t = threadIdx.x, lane = t & 63;
  const int w = __builtin_amdgcn_readfirstlane(t >> 6);
  const int fq = w & 3, kh = w >> 2;
  const bool has3 = 8 + fq < cnt;
  const int perm = 2 * kh + (fq & 1);
  const int row0 = rt * 64;
  const int dq = fg;
  const u16* hT = (const u16*)(ws + OFF_HT);
  const int oct = t & 7, dr = t >> 3;
  const int g = lane >> 4, li = lane & 15, qq = li >> 2, p = lane & 3;
  float* SP = (float*)(smem + K3_SOFF);
  const int r0 = 8 * g + qq;
  const int sw0 = k3_swz(r0) * 32;
  const char* base0 = smem + K3_HOFF + r0 * 128 + p * 8;
  const int T0 = fstart + 2 * fq, T2 = fstart + (has3 ? 8 + fq : 0);
  f32x4 acc[3][4] = {};
  u32x4 hres;
  unsigned epoch;
  const int pg = w < 3 ? 2 * w : w == 3 ? 1 : w == 4 ? 3 : w;
  const int kb0 = pg < 6 ? 11 * (pg >> 1) + 6 * (pg & 1) : 33 + 5 * (pg - 6);
  const bool six = pg < 6 && !(pg & 1);
  const u32x4* Ap = (const u32x4*)(ws + OFF_W2) + ((size_t)(4 * dq) * KSF + kb0) * 64 + lane;
  u32x4 av[6][4];
  const int k5 = six ? 5 : 4;
  {
    u32x4 hv[8];
#pragma unroll
    for (int i = 0; i < 8; ++i) hv[i] = *(const u32x4*)(hT + (size_t)rt * 32768 + (t + 512 * i) * 8);
    const u32x4* W1a = (const u32x4*)(ws + OFF_W1) + (T0 * 16 + 8 * kh) * 64 + lane;
    const u32x4* W1c = (const u32x4*)(ws + OFF_W1) + (T2 * 16 + 8 * kh) * 64 + lane;
    f32x4 cpart;
    u32x2 bpart;
    const int cf = min(16 * fstart + 4 * (t & 63), F_ - 4);
    {
      const float* cp = (const float*)(ws + OFF_CS) + (2 * w) * F_ + cf;
      cpart = *(const f32x4*)cp + *(const f32x4*)(cp + F_);
      bpart = *(const u32x2*)(b1 + cf);
    }
    epoch = __hip_atomic_load((gu32*)(ws + OFF_EP), RLX_AGENT);
    u32x4 wv[3][8];
    SB();
    char* hdst = smem + K3_HOFF + dr * 128 + (((oct >> 1) ^ k3_swz(dr)) * 32) + (oct & 1) * 16;
#pragma unroll
    for (int i = 0; i < 8; ++i) *(u32x4*)(hdst + i * (64 * 128)) = hv[i];
    if (lane < 48) {
      *(f32x4*)(smem + K3_COFF + 1536 + w * 768 + lane * 16) = cpart;
      if (w == 0) *(f32x4*)(smem + K3_COFF + 768 + lane * 16) = (f32x4){bflo(bpart[0]), bfhi(bpart[0]), bflo(bpart[1]), bfhi(bpart[1])};
    }
    SB();
#pragma unroll
    for (int ks = 0; ks < W1_PFD; ++ks) { wv[0][ks] = W1a[ks * 64]; wv[1][ks] = W1a[(16 + ks) * 64]; wv[2][ks] = W1c[ks * 64]; }
    SB();
    __syncthreads();
    if (w == 7 && lane < 48) {
      const f32x4* cp = (const f32x4*)(smem + K3_COFF + 1536) + lane;
      *(f32x4*)(smem + K3_COFF + lane * 16) = ((cp[0] + cp[48]) + (cp[96] + cp[144])) + ((cp[192] + cp[240]) + (cp[288] + cp[336]));
    }
    const u32x4 ones = {0x3f803f80u, 0x3f803f80u, 0x3f803f80u, 0x3f803f80u};
    f32x4 st0 = {}, st1 = {};
    const char* basek = base0 + kh * (8 * 4096);
    const char* basew = basek + ((fq * 32) ^ sw0);
    const int swp = sw0 ^ (perm * 32);
    bf16x8 B[2][4], fr[2];
    auto loadB = [&](int kk, int bf) {
#pragma unroll
      for (int lt = 0; lt < 4; ++lt) {
        const char* a = basek + kk * 4096 + ((lt * 32) ^ swp);
        const u32x2 v0 = trread(a), v1 = trread(a + 512);
        B[bf][lt] = asbf((u32x4){v0[0], v0[1], v1[0], v1[1]});
      }
      const u32x2 s0 = trread(basew + kk * 4096), s1 = trread(basew + kk * 4096 + 512);
      fr[bf] = asbf((u32x4){s0[0], s0[1], s1[0], s1[1]});
    };
    loadB(0, 0);
#pragma unroll
    for (int kk = 0; kk < 8; ++kk) {
      if (kk + W1_PFD < 8) {
        wv[0][kk + W1_PFD] = W1a[(kk + W1_PFD) * 64];
        wv[1][kk + W1_PFD] = W1a[(16 + kk + W1_PFD) * 64];
        wv[2][kk + W1_PFD] = W1c[(kk + W1_PFD) * 64];
      }
      if (kk >= 8 - PF_LOOP) {
#pragma unroll
        for (int i = 0; i < 4; ++i) av[kk - (8 - PF_LOOP)][i] = Ap[(i * KSF + kk - (8 - PF_LOOP)) * 64];
      }
      if (kk < 7) loadB(kk + 1, (kk + 1) & 1);
      SB();
      st0 = __builtin_amdgcn_mfma_f32_16x16x32_bf16(asbf(ones), fr[kk & 1], st0, 0, 0, 0);
      st1 = __builtin_amdgcn_mfma_f32_16x16x32_bf16(fr[kk & 1], fr[kk & 1], st1, 0, 0, 0);
#pragma unroll
      for (int n = 0; n < 3; ++n) {
#pragma unroll
        for (int lt = 0; lt < 4; ++lt)
          acc[n][lt] = __builtin_amdgcn_mfma_f32_16x16x32_bf16(asbf(wv[n][kk]), B[kk & 1][lt], acc[n][lt], 0, 0, 0);
      }
      SB();
    }
#pragma unroll
    for (int k = PF_LOOP; k < PF_POST; ++k)
#pragma unroll
      for (int i = 0; i < 4; ++i) av[k][i] = Ap[(i * KSF + k) * 64];
    SB();
    {
      const char* a = base0 + (2 * dq + (w & 1)) * 4096 + (((w >> 1) * 32) ^ sw0);
      const u32x2 v0 = trread(a), v1 = trread(a + 512);
      hres = (u32x4){v0[0], v0[1], v1[0], v1[1]};
    }
    if (g == qq) {
      SP[kh * 128 + 16 * fq + li] = st0[0];
      SP[kh * 128 + 64 + 16 * fq + li] = p == 0 ? st1[0] : p == 1 ? st1[1] : p == 2 ? st1[2] : st1[3];
    }
    f32x4* X = (f32x4*)(smem + K3_XOFF) + w * (8 * 64) + lane;
    const int o1 = (fq & 1) * 64;
    X[0 * 64 + o1] = acc[0][2]; X[1 * 64 - o1] = acc[0][3];
    X[2 * 64 + o1] = acc[1][2]; X[3 * 64 - o1] = acc[1][3];
#pragma unroll
    for (int lt = 1; lt < 4; ++lt) X[(4 + (lt ^ perm)) * 64] = acc[2][lt];
  }
  SB();
  __syncthreads();
  {
    const __amdgpu_buffer_rsrc_t rh = __builtin_amdgcn_make_buffer_rsrc(ws + OFF_HH, 0, L_ * F_ * 2, 0x00020000);
    const f32x4* XB = (const f32x4*)(smem + K3_XOFF) + lane;
    const f32x4* X = XB + (w ^ 4) * (8 * 64);
    const float* cb0 = (const float*)(smem + K3_COFF) + 32 * fq + 4 * g;
    float mm[2], ss[2];
#pragma unroll
    for (int h2 = 0; h2 < 2; ++h2) {
      const int l = 16 * (h2 ^ perm) + li;
      const float m = (SP[l] + SP[128 + l]) * (1.0f / D_);
      const float var = fmaxf((SP[64 + l] + SP[192 + l]) * (1.0f / D_) - m * m, 0.0f);
      const float r1 = __builtin_amdgcn_rsqf(var + 1e-5f);
      const float r2 = __builtin_amdgcn_rsqf(var * r1 * r1 + 1e-5f);
      mm[h2] = m; ss[h2] = r1 * r2;
    }
#pragma unroll
    for (int h2 = 0; h2 < 2; ++h2) {
      const int lt = h2 ^ perm, par = (h2 ^ fq) & 1;
      const u32x2 g0 = gelu4(acc[0][h2] + X[par * 64], cb0, mm[h2], ss[h2]);
      const u32x2 g1 = gelu4(acc[1][h2] + X[(2 + par) * 64], cb0 + 16, mm[h2], ss[h2]);
      __builtin_amdgcn_raw_buffer_store_b128((u32x4){g0[0], g0[1], g1[0], g1[1]}, rh,
                                             (((4 * rt + lt) * KSF + (T0 >> 1)) * 64 + lane) * 16, 0, 16);
      if (PF_POST + h2 < 6) {
#pragma unroll
        for (int i = 0; i < 4; ++i) av[PF_POST + h2][i] = Ap[(i * KSF + (PF_POST + h2 < 5 ? PF_POST + h2 : k5)) * 64];
      }
      SB();
    }
    if (has3) {
      const f32x4* Xo0 = XB + (w ^ 1) * (8 * 64);
      const f32x4* Xo1 = XB + (w ^ 5) * (8 * 64);
      const f32x4 aown = acc[2][0] + X[(4 + perm) * 64], aoth = Xo0[(4 + perm) * 64] + Xo1[(4 + perm) * 64];
      const bool odd = fq & 1;
      const float* cb2 = (const float*)(smem + K3_COFF) + 16 * (8 + (fq & 2)) + 4 * g;
      const u32x2 g0 = gelu4(odd ? aoth : aown, cb2, mm[0], ss[0]);
      const u32x2 g1 = gelu4(odd ? aown : aoth, cb2 + 16, mm[0], ss[0]);
      __builtin_amdgcn_raw_buffer_store_b128((u32x4){g0[0], g0[1], g1[0], g1[1]}, rh,
                                             (((4 * rt + perm) * KSF + (T2 >> 1)) * 64 + lane) * 16, 0, 16);
    }
  }
  asm volatile("s_waitcnt vmcnt(0)" ::: "memory");
  __builtin_amdgcn_s_barrier();
  gu64* flags = (gu64*)(ws + OFF_FL) + rt * (8 * 16);
  const unsigned tag = __builtin_amdgcn_readfirstlane(epoch);
  const unsigned long long fval = ((unsigned long long)tag << 32) | (unsigned)~tag;
  if (t == 0) __hip_atomic_store(flags + fg * 16, fval, RLX_AGENT);
#pragma unroll
  for (int k = PF_POST + 2; k < 6; ++k)
#pragma unroll
    for (int i = 0; i < 4; ++i) av[k][i] = Ap[(i * KSF + (k < 5 ? k : k5)) * 64];
  const u32x4 bias = *(const u32x4*)(b2 + 64 * dq + 32 * (w & 1) + 8 * g);
  SB();
  for (unsigned spins = 0; spins < (1u << 22); ++spins) {
    if (__all(__hip_atomic_load(flags + pg * 16, RLX_AGENT) == fval)) break;
    __builtin_amdgcn_s_sleep(1);
  }
  __builtin_amdgcn_fence(__ATOMIC_ACQUIRE, "wavefront");
  f32x4 oc[4][4] = {};
  {
    const __amdgpu_buffer_rsrc_t rs = __builtin_amdgcn_make_buffer_rsrc(ws + OFF_HH, 0, L_ * F_ * 2, 0x00020000);
    const int boff = (((4 * rt) * KSF + kb0) * 64 + lane) * 16;
    constexpr int NB = 3;
    u32x4 bv[NB][4];
#pragma unroll
    for (int k = 0; k < NB; ++k)
#pragma unroll
      for (int i = 0; i < 4; ++i) bv[k][i] = __builtin_amdgcn_raw_buffer_load_b128(rs, boff + (i * KSF + k) * 1024, 0, 16);
    SB();
#pragma unroll
    for (int k = 0; k < 6; ++k) {
      if (k < 5 || six) {
#pragma unroll
        for (int a = 0; a < 4; ++a)
#pragma unroll
          for (int bb = 0; bb < 4; ++bb)
            oc[a][bb] = __builtin_amdgcn_mfma_f32_16x16x32_bf16(asbf(av[k][a]), asbf(bv[k % NB][bb]), oc[a][bb], 0, 0, 0);
      }
      if (k + NB < 6) {
        const int kn = k + NB < 5 ? k + NB : k5;
#pragma unroll
        for (int i = 0; i < 4; ++i)
          bv[k % NB][i] = __builtin_amdgcn_raw_buffer_load_b128(rs, boff + (i * KSF + kn) * 1024, 0, 16);
      }
      SB();
    }
  }
  f32x4* R = (f32x4*)(smem + K3_ROFF);
#pragma unroll
  for (int bb = 0; bb < 4; ++bb)
#pragma unroll
    for (int a = 0; a < 4; ++a) R[((w * 4 + bb) * 4 + a) * 64 + lane] = oc[a][bb];
  __syncthreads();
  f32x4 sum[2];
#pragma unroll
  for (int a = 0; a < 2; ++a) {
    const f32x4* Rp = R + (((w >> 1)) * 4 + 2 * (w & 1) + a) * 64 + lane;
    f32x4 s0 = Rp[0 * 1024] + Rp[1 * 1024], s1 = Rp[2 * 1024] + Rp[3 * 1024];
    f32x4 s2 = Rp[4 * 1024] + Rp[5 * 1024], s3 = Rp[6 * 1024] + Rp[7 * 1024];
    sum[a] = (s0 + s1) + (s2 + s3);
  }
  const int lrow = row0 + 16 * (w >> 1) + li;
  u32x4 o;
#pragma unroll
  for (int hf = 0; hf < 2; ++hf) {
#pragma unroll
    for (int k = 0; k < 2; ++k) {
      const unsigned hb = hres[2 * hf + k], bb = bias[2 * hf + k];
      o[2 * hf + k] = pack2(bflo(hb) + (sum[hf][2 * k] + bflo(bb)), bfhi(hb) + (sum[hf][2 * k + 1] + bfhi(bb)));
    }
  }
  *(u32x4*)(out + (size_t)lrow * D_ + 64 * dq + 32 * (w & 1) + 8 * g) = o;
}

extern "C" void kernel_launch(void* const* d_in, const int* in_sizes, int n_in, void* d_out, int out_size, void* d_ws,
                              size_t ws_size, hipStream_t stream) {
  const u16* x = (const u16*)d_in[0];
  const u16* la = (const u16*)d_in[1];
  const u16* ai = (const u16*)d_in[2];
  const u16* cr = (const u16*)d_in[3];
  const u16* ci = (const u16*)d_in[4];
  const u16* ldt = (const u16*)d_in[5];
  const u16* dsk = (const u16*)d_in[6];
  const u16* w1 = (const u16*)d_in[8];
  const u16* b1 = (const u16*)d_in[9];
  const u16* w2 = (const u16*)d_in[10];
  const u16* b2 = (const u16*)d_in[11];
  char* ws = (char*)d_ws;
  k2_conv<<<256, 512, 0, stream>>>(x, la, ai, cr, ci, ldt, dsk, w1, w2, ws);
  k34_mlp<<<256, 512, 0, stream>>>(b1, b2, ws, (u16*)d_out);
}
```

```cpp
#include <hip/hip_runtime.h>
#include <hip/hip_bf16.h>
#include <stdint.h>

typedef __attribute__((ext_vector_type(8))) __bf16 bf16x8;
typedef __attribute__((ext_vector_type(2))) __bf16 bf16x2;
typedef __attribute__((ext_vector_type(8))) _Float16 f16x8;
typedef __attribute__((ext_vector_type(2))) _Float16 f16x2;
typedef __attribute__((ext_vector_type(4))) float f32x4;
typedef __attribute__((ext_vector_type(2))) float f32x2;
typedef __attribute__((ext_vector_type(16))) float f32x16;
typedef __attribute__((ext_vector_type(4))) unsigned u32x4;
typedef __attribute__((ext_vector_type(2))) unsigned u32x2;
typedef __attribute__((ext_vector_type(4))) short s16x4;
typedef unsigned short u16;

#define LDS_AS __attribute__((address_space(3)))
#define SB() __builtin_amdgcn_sched_barrier(0)
constexpr int NREP = 8;
constexpr int POLL_SLEEP = 4;

constexpr int L_ = 2048, D_ = 512, NS_ = 128, F_ = 1376;
constexpr int KSF = F_ / 32;

constexpr size_t OFF_EP = 0;
constexpr size_t OFF_FL = 128u << 10;
constexpr size_t OFF_CS = 256u << 10;
constexpr size_t OFF_F1 = 512u << 10;
constexpr size_t OFF_HT = 4u << 20;
constexpr size_t OFF_UX = 8u << 20;
constexpr size_t OFF_W1 = 13u << 20;
constexpr size_t OFF_W2 = 15u << 20;
constexpr size_t OFF_HH = 18u << 20;
typedef __attribute__((address_space(1))) unsigned gu32;
typedef __attribute__((address_space(1))) unsigned long long gu64;
#define RLX_AGENT __ATOMIC_RELAXED, __HIP_MEMORY_SCOPE_AGENT

constexpr int KIMG = 2112;
constexpr int KIMG_B = KIMG * 2;

__device__ __forceinline__ unsigned pack2(float a, float b) {
  f32x2 v = {a, b};
  bf16x2 r = __builtin_convertvector(v, bf16x2);
  return __builtin_bit_cast(unsigned, r);
}
__device__ __forceinline__ unsigned pack2h(float a, float b) {
  f32x2 v = {a, b};
  f16x2 r = __builtin_convertvector(v, f16x2);
  return __builtin_bit_cast(unsigned, r);
}
__device__ __forceinline__ float bflo(unsigned u) { return __builtin_bit_cast(float, u << 16); }
__device__ __forceinline__ float bfhi(unsigned u) { return __builtin_bit_cast(float, u & 0xffff0000u); }
__device__ __forceinline__ float bf2f(u16 u) { return __builtin_bit_cast(float, (unsigned)u << 16); }
__device__ __forceinline__ float bfround(float a) { return bflo(pack2(a, a)); }
__device__ __forceinline__ bf16x8 asbf(u32x4 v) { return __builtin_bit_cast(bf16x8, v); }
__device__ __forceinline__ f16x8 ash(u32x4 v) { return __builtin_bit_cast(f16x8, v); }
__device__ __forceinline__ u32x2 trread(const void* p) {
  s16x4 v = __builtin_amdgcn_ds_read_tr16_b64_v4i16((LDS_AS s16x4*)(p));
  return __builtin_bit_cast(u32x2, v);
}
template <int CTRL, int RMASK>
__device__ __forceinline__ float dppf(float idv, float v) {
  return __builtin_bit_cast(float, __builtin_amdgcn_update_dpp(__builtin_bit_cast(int, idv), __builtin_bit_cast(int, v),
                                                               CTRL, RMASK, 0xf, false));
}
__device__ __forceinline__ float wave_sum(float v) {
  v += dppf<0x111, 0xf>(0.f, v);
  v += dppf<0x112, 0xf>(0.f, v);
  v += dppf<0x114, 0xf>(0.f, v);
  v += dppf<0x118, 0xf>(0.f, v);
  v += dppf<0x142, 0xa>(0.f, v);
  v += dppf<0x143, 0xc>(0.f, v);
  return __builtin_bit_cast(float, __builtin_amdgcn_readlane(__builtin_bit_cast(int, v), 63));
}
__device__ __forceinline__ float wave_max(float v) {
  const float ni = -__builtin_inff();
  v = fmaxf(v, dppf<0x111, 0xf>(ni, v));
  v = fmaxf(v, dppf<0x112, 0xf>(ni, v));
  v = fmaxf(v, dppf<0x114, 0xf>(ni, v));
  v = fmaxf(v, dppf<0x118, 0xf>(ni, v));
  v = fmaxf(v, dppf<0x142, 0xa>(ni, v));
  v = fmaxf(v, dppf<0x143, 0xc>(ni, v));
  return __builtin_bit_cast(float, __builtin_amdgcn_readlane(__builtin_bit_cast(int, v), 63));
}

constexpr int UCOPY_B = 4640;
constexpr int K2_KOFF = 0;
constexpr int K2_UOFF = 2 * 2 * KIMG_B;
constexpr int K2_UCH = 4 * UCOPY_B;
constexpr int K2_YOFF = K2_UOFF + 2 * K2_UCH;
constexpr int K2_MOFF = K2_YOFF + 2 * 2048 * 4;
constexpr int K2_TOFF = K2_MOFF + 64;
constexpr int K2_FOFF = K2_TOFF + 8 * 24 * 32 * 4;
constexpr int FSTR = 128 * 4 + 16;
constexpr int F_B1 = 0, F_B2 = 4, F_A1 = 12, F_A2 = 20, F_N = 29;
constexpr int K2_Y2OFF = K2_FOFF;
constexpr int K2_LDS = K2_FOFF + 32768;

__device__ __forceinline__ f32x2 cmul(f32x2 a, f32x2 b) { return (f32x2){a.x * b.x - a.y * b.y, a.x * b.y + a.y * b.x}; }
__device__ __forceinline__ unsigned cmulh(unsigned ua, unsigned ub) {
  const f16x2 a = __builtin_bit_cast(f16x2, ua), b = __builtin_bit_cast(f16x2, ub);
  const f16x2 t = a * (f16x2){b.x, b.x};
  const f16x2 r = __builtin_elementwise_fma((f16x2){-a.y, a.x}, (f16x2){b.y, b.y}, t);
  return __builtin_bit_cast(unsigned, r);
}
__device__ __forceinline__ f32x2 cexp2r(float x, float y) {
  const float e = __builtin_amdgcn_exp2f(x), th = __builtin_amdgcn_fractf(y);
  return (f32x2){e * __builtin_amdgcn_cosf(th), e * __builtin_amdgcn_sinf(th)};
}

struct TOff { u16 v[2064]; };
constexpr TOff make_toff() {
  TOff t{};
  for (int q = 0; q < 2064; ++q) {
    const int lag = q <= 2047 ? 2047 - q : 0;
    const int s = lag >= 1024 ? 3 : lag >= 512 ? 2 : lag >= 256 ? 1 : 0;
    const int pp = s ? ((lag + (1 << (s - 1)) - 1 + ((lag >> s) & 1)) >> s) << s : lag;
    const int r2 = pp >> 8, rp = pp & 7, col = (pp & 255) >> 3;
    const int row = r2 == 0 ? rp : r2 == 1 ? 8 + (rp >> 1) : r2 < 4 ? 8 + 2 * r2 + (rp >> 2) : 12 + r2;
    t.v[q] = (u16)((row * 32 + col) * 4);
  }
  return t;
}
__device__ __attribute__((aligned(16))) const TOff g_toff = make_toff();

template <int TS>
__device__ __forceinline__ void conv_main(char* smem, int ch, int rh, int lane) {
  constexpr int NF = TS == 0 ? 8 : 6;
  const int j = lane & 15, gq = lane >> 4;
  const char* abase = smem + K2_KOFF + ch * (2 * KIMG_B) + ((j & 1) ? 0 : KIMG_B) + 2 * (2072 - 8 * (j >> 1) + 8 * gq) -
                      64 * (4 * rh + 3) - 512 * 7;
  const char* bbase = smem + K2_UOFF + ch * K2_UCH + (j & 3) * UCOPY_B + 2 * (232 + 64 * (j >> 2) + 8 * gq) -
                      64 * (4 * rh + 3);
  f32x4 acc[8] = {};
  bf16x8 A[2][NF], B[2][NF];
#pragma unroll
  for (int a = 0; a < NF; ++a) {
    A[0][a] = *(const bf16x8*)(abase + 512 * (7 - a) + 64 * 3);
    B[0][a] = *(const bf16x8*)(bbase + 512 * a + 64 * 3);
  }
#pragma unroll
  for (int rr = 0; rr < 4; ++rr) {
    if (rr < 3) {
#pragma unroll
      for (int a = 0; a < NF; ++a) {
        A[(rr + 1) & 1][a] = *(const bf16x8*)(abase + 512 * (7 - a) + 64 * (2 - rr));
        B[(rr + 1) & 1][a] = *(const bf16x8*)(bbase + 512 * a + 64 * (2 - rr));
      }
    }
    SB();
#pragma unroll
    for (int a = 0; a < NF; ++a)
#pragma unroll
      for (int b = 0; b < NF; ++b) {
        const int nt = a + b;
        const bool use = TS == 0 ? (nt == 7 || nt == 6 || nt == 2) : (nt <= 5 && nt != 2);
        if (use) acc[nt] = __builtin_amdgcn_mfma_f32_16x16x32_bf16(A[rr & 1][a], B[rr & 1][b], acc[nt], 0, 0, 0);
      }
    SB();
  }
  float* Y = (float*)(smem + K2_Y2OFF) + (rh * 2 + ch) * 2048 + 16 * gq + 2 * (j & 3) + 64 * (j >> 2);
#pragma unroll
  for (int nt = 0; nt < 8; ++nt) {
    const bool use = TS == 0 ? (nt == 7 || nt == 6 || nt == 2) : (nt <= 5 && nt != 2);
    if (use) {
      *(f32x2*)(Y + 256 * nt) = (f32x2){acc[nt][0], acc[nt][1]};
      *(f32x2*)(Y + 256 * nt + 8) = (f32x2){acc[nt][2], acc[nt][3]};
    }
  }
}

__global__ __launch_bounds__(512, 2) void k2_conv(const u16* __restrict__ x, const u16* __restrict__ la,
                                               const u16* __restrict__ ai, const u16* __restrict__ cr,
                                               const u16* __restrict__ ci, const u16* __restrict__ ldt,
                                               const u16* __restrict__ dskip, const u16* __restrict__ w1,
                                               const u16* __restrict__ w2, char* __restrict__ ws) {
  __shared__ __attribute__((aligned(16))) char smem[K2_LDS];
  const int t = threadIdx.x, lane = t & 63;
  const int w = __builtin_amdgcn_readfirstlane(t >> 6);
  const int blk = blockIdx.x;
  const int ch = w >> 2;
  const int d = 2 * blk + ch;
  float* SC = (float*)(smem + K2_MOFF);
  float* T = (float*)(smem + K2_TOFF);
  u32x4 xrow[2];
#pragma unroll
  for (int i = 0; i < 2; ++i) xrow[i] = *(const u32x4*)(x + (size_t)(16 * (blk >> 1) + 2 * w + i) * D_ + lane * 8);
  const unsigned epoch = __hip_atomic_load((gu32*)(ws + OFF_EP), RLX_AGENT);
  const int half = (w >> 1) & 1, n = t & 127;
  const int pi = d * NS_ + n;
  const u16 p_la = la[pi], p_ai = ai[pi], p_dt = ldt[d];
  u16 p_cr = 0, p_ci = 0, q_cr = 0, q_ci = 0;
  if (half == 0) { p_cr = cr[pi]; p_ci = ci[pi]; q_cr = cr[pi ^ 64]; q_ci = ci[pi ^ 64]; }
  const int c = t >> 8, v = t & 255;
  const float dsk = bf2f(dskip[2 * blk + (t >> 8)]);
  const u32x4 tof = *(const u32x4*)(g_toff.v + 8 * v);
  const unsigned tof8 = g_toff.v[8 * v + 8];
  const int role = w & 3;
  SB();
  const int g4 = lane >> 4, qq = (lane & 15) >> 2, p4 = lane & 3;
  {
    float s1[2], s2[2];
#pragma unroll
    for (int i = 0; i < 2; ++i) {
      s1[i] = 0.f; s2[i] = 0.f;
#pragma unroll
      for (int j = 0; j < 4; ++j) {
        const float a = bflo(xrow[i][j]), b = bfhi(xrow[i][j]);
        s1[i] += a + b;
        s2[i] += a * a + b * b;
      }
    }
#pragma unroll
    for (int i = 0; i < 2; ++i) { s1[i] = wave_sum(s1[i]) * (1.0f / D_); s2[i] = wave_sum(s2[i]) * (1.0f / D_); }
    char* S = smem + K2_TOFF;
    if ((lane >> 5) == (blk & 1)) {
#pragma unroll
      for (int i = 0; i < 2; ++i) {
        const float rs = __builtin_amdgcn_rsqf(fmaxf(s2[i] - s1[i] * s1[i], 0.0f) + 1e-5f);
        u32x4 uv;
#pragma unroll
        for (int j = 0; j < 4; ++j) uv[j] = pack2((bflo(xrow[i][j]) - s1[i]) * rs, (bfhi(xrow[i][j]) - s1[i]) * rs);
        *(u32x4*)(S + (2 * w + i) * 544 + (lane & 31) * 16) = uv;
        *(u32x4*)(S + 8704 + (2 * w + i) * 544 + (lane & 31) * 16) = xrow[i];
      }
    }
    __syncthreads();
    const __amdgpu_buffer_rsrc_t rux = __builtin_amdgcn_make_buffer_rsrc(ws + OFF_UX, 0, D_ * 8192, 0x00020000);
    const char* a = S + (g4 >> 1) * 8704 + (8 * (g4 & 1) + qq) * 544 + (32 * w + 4 * p4) * 2;
#pragma unroll
    for (int r = 0; r < 2; ++r) {
      const u32x2 v0 = trread(a + r * 32), v1 = trread(a + r * 32 + 4 * 544);
      const int dd = 256 * (blk & 1) + 32 * w + 16 * r + (lane & 15);
      __builtin_amdgcn_raw_buffer_store_b128((u32x4){v0[0], v0[1], v1[0], v1[1]}, rux, dd * 8192 + (blk >> 1) * 64 + g4 * 16, 0, 16);
    }
  }
  {
    const float dt = bfround(__builtin_amdgcn_exp2f(bf2f(p_dt) * 1.4426950408889634f));
    const float are = -bfround(__builtin_amdgcn_exp2f(bf2f(p_la) * 1.4426950408889634f));
    const float ax = (are * dt) * 1.4426950408889634f, ay = (bf2f(p_ai) * dt) * 0.15915494309189535f;
    char* F = smem + K2_FOFF + ch * (F_N * FSTR) + n * 4;
    auto put = [&](int e, f32x2 z) { *(unsigned*)(F + e * FSTR) = pack2h(z.x, z.y); };
    if (half == 0) {
      const float mx = wave_max(fmaxf(fmaxf(fabsf(bf2f(p_cr)), fabsf(bf2f(p_ci))), fmaxf(fabsf(bf2f(q_cr)), fabsf(bf2f(q_ci)))));
      const unsigned eb = min((__builtin_bit_cast(unsigned, mx) >> 23) + 2u, 252u);
      const float inv = __builtin_bit_cast(float, (254u - eb) << 23);
      if (n == 0) SC[8 + ch] = 2.0f * dt * __builtin_bit_cast(float, eb << 23);
      const f32x2 z1 = cexp2r(ax, -ay), z8 = cexp2r(8.0f * ax, 8.0f * ay);
      const f32x2 z16 = cmul(z8, z8);
      put(F_B1 + 0, (f32x2){1.0f, 0.0f});
      put(F_B1 + 1, z8);
      put(F_B1 + 2, z16);
      put(F_B1 + 3, cmul(z16, z8));
      f32x2 a1[8];
      a1[0] = (f32x2){bf2f(p_cr) * inv, -bf2f(p_ci) * inv};
      const f32x2 z2 = cmul(z1, z1), z4 = cmul(z2, z2);
      a1[1] = cmul(a1[0], z1); a1[2] = cmul(a1[0], z2); a1[3] = cmul(a1[1], z2);
#pragma unroll
      for (int k = 0; k < 4; ++k) a1[4 + k] = cmul(a1[k], z4);
#pragma unroll
      for (int k = 0; k < 8; ++k) put(F_A1 + k, a1[k]);
    } else {
      const f32x2 y1 = cexp2r(32.0f * ax, 32.0f * ay), x1 = cexp2r(256.0f * ax, -256.0f * ay);
      const f32x2 y2 = cmul(y1, y1), y4 = cmul(y2, y2), y3 = cmul(y2, y1);
      put(F_B2 + 0, (f32x2){1.0f, 0.0f});
      put(F_B2 + 1, y1); put(F_B2 + 2, y2); put(F_B2 + 3, y3); put(F_B2 + 4, y4);
      put(F_B2 + 5, cmul(y4, y1)); put(F_B2 + 6, cmul(y4, y2)); put(F_B2 + 7, cmul(y4, y3));
      const f32x2 x2 = cmul(x1, x1), x4 = cmul(x2, x2), x3 = cmul(x2, x1);
      put(F_A2 + 0, (f32x2){1.0f, 0.0f});
      put(F_A2 + 1, x1); put(F_A2 + 2, x2); put(F_A2 + 3, x3); put(F_A2 + 4, x4);
      put(F_A2 + 5, cmul(x4, x1)); put(F_A2 + 6, cmul(x4, x2)); put(F_A2 + 7, cmul(x4, x3)); put(F_A2 + 8, cmul(x4, x4));
      const int z = (ch << 7) | n;
      if (z < 200) {
        const int cc = z / 100, rem = z - 100 * cc, cix = rem / 25, m = rem - 25 * cix;
        *(u32x4*)(smem + K2_UOFF + cc * K2_UCH + cix * UCOPY_B + m * 16) = (u32x4){0u, 0u, 0u, 0u};
      } else if (z < 232) {
        const int i = z - 200;
        *(u32x4*)(smem + K2_KOFF + (i >> 3) * KIMG_B + (256 + (i & 7)) * 16) = (u32x4){0u, 0u, 0u, 0u};
      }
    }
  }
  asm volatile("s_waitcnt vmcnt(0)" ::: "memory");
  __syncthreads();
  const unsigned tag = __builtin_amdgcn_readfirstlane(epoch) + 1u;
  const unsigned long long fval = ((unsigned long long)tag << 32) | (unsigned)~tag;
  if (t < NREP) __hip_atomic_store((gu64*)(ws + OFF_F1) + t * 512 + blk, fval, RLX_AGENT);
  const int gw = (w >= 1 && w <= 6) ? blk + 256 * (w - 1) : 1376;
  u32x4 wl[2];
  if (gw < 1376) {
    const u16* src;
    int rs;
    if (gw < 688) { src = w1 + (size_t)(32 * (gw & 15)) * F_ + 32 * (gw >> 4); rs = F_; }
    else { const int u2 = gw - 688, ap = u2 / KSF, kb = u2 - ap * KSF; src = w2 + (size_t)(32 * kb) * D_ + 32 * ap; rs = D_; }
    src += (lane >> 2) * rs + (lane & 3) * 8;
    wl[0] = *(const u32x4*)src;
    wl[1] = *(const u32x4*)(src + 16 * rs);
  }
  SB();
  {
    const int kq = w & 3;
    const int r = lane & 31, hh = lane >> 5;
    const int r1 = r < 8 ? r : r < 12 ? 2 * (r - 8) : r < 16 ? 4 * (r & 1) : 0;
    const int r2 = r < 8 ? 0 : r < 12 ? 1 : r < 16 ? 2 + ((r - 12) >> 1) : r < 21 ? r - 12 : 0;
    const char* F = smem + K2_FOFF + ch * (F_N * FSTR) + (kq * 32 + 4 * hh) * 4;
    const char* fb1 = F + (F_B1 + (r & 3)) * FSTR;
    const char* fb2 = F + (F_B2 + (r >> 2)) * FSTR;
    const char* fa1 = F + (F_A1 + r1) * FSTR;
    const char* fa2 = F + (F_A2 + r2) * FSTR;
    f32x16 acc = {};
#pragma unroll
    for (int ks = 0; ks < 4; ++ks) {
      const u32x4 b1v = *(const u32x4*)(fb1 + ks * 32), b2v = *(const u32x4*)(fb2 + ks * 32);
      const u32x4 a1v = *(const u32x4*)(fa1 + ks * 32), a2v = *(const u32x4*)(fa2 + ks * 32);
      u32x4 av, bv;
#pragma unroll
      for (int q = 0; q < 4; ++q) {
        av[q] = cmulh(a1v[q], a2v[q]);
        bv[q] = cmulh(b1v[q], b2v[q]);
      }
      acc = __builtin_amdgcn_mfma_f32_32x32x16_f16(ash(av), ash(bv), acc, 0, 0, 0);
    }
    float* Tw = T + w * (24 * 32);
#pragma unroll
    for (int g = 0; g < 12; ++g) Tw[((g & 3) + 8 * (g >> 2) + 4 * hh) * 32 + r] = acc[g];
  }
  if (w == 0) {
    const gu64* fp = (const gu64*)(ws + OFF_F1) + (blk % NREP) * 512 + lane;
    unsigned long long fa[3][4];
#pragma unroll
    for (int k = 0; k < 2; ++k) {
#pragma unroll
      for (int i = 0; i < 4; ++i) fa[k][i] = __hip_atomic_load(fp + 64 * i, RLX_AGENT);
      __builtin_amdgcn_s_sleep(POLL_SLEEP);
    }
    bool done = false;
    for (unsigned spins = 0; spins < (1u << 16) && !done; ++spins) {
#pragma unroll
      for (int k = 0; k < 3; ++k) {
        if (!done) {
#pragma unroll
          for (int i = 0; i < 4; ++i) fa[(k + 2) % 3][i] = __hip_atomic_load(fp + 64 * i, RLX_AGENT);
          done = __all((fa[k][0] == fval) & (fa[k][1] == fval) & (fa[k][2] == fval) & (fa[k][3] == fval));
        }
      }
    }
  }
  __syncthreads();
  u32x4 ua, ub, xa;
  {
    const __amdgpu_buffer_rsrc_t rux = __builtin_amdgcn_make_buffer_rsrc(ws + OFF_UX + (size_t)blk * 16384, 0, 16384, 0x00020000);
    int o = (t >> 1) * 64 + (t & 1) * 16;
    asm volatile("" : "+v"(o)::"memory");
    ua = __builtin_amdgcn_raw_buffer_load_b128(rux, o, 0, 16);
    xa = __builtin_amdgcn_raw_buffer_load_b128(rux, o + 32, 0, 16);
    ub = __builtin_amdgcn_raw_buffer_load_b128(rux, v < 255 ? o + ((t & 1) ? 48 : 16) : 1 << 20, 0, 16);
  }
  SB();
  {
    const char* Tq = smem + K2_TOFF + c * 4 * (24 * 32 * 4);
    const float sc = SC[8 + c];
    float kv[9];
#pragma unroll
    for (int jj = 0; jj < 9; ++jj) {
      const char* a = Tq + (jj == 8 ? tof8 : (jj & 1) ? (tof[jj >> 1] >> 16) : (tof[jj >> 1] & 0xffffu));
      kv[jj] = ((*(const float*)a + *(const float*)(a + 3072)) + (*(const float*)(a + 6144) + *(const float*)(a + 9216))) * sc;
    }
    if (v == 255) kv[8] = 0.0f;
    u32x4 c0, c1;
#pragma unroll
    for (int jj = 0; jj < 4; ++jj) {
      c0[jj] = pack2(kv[2 * jj], kv[2 * jj + 1]);
      c1[jj] = pack2(kv[2 * jj + 1], kv[2 * jj + 2]);
    }
    char* dst = smem + K2_KOFF + c * (2 * KIMG_B) + v * 16;
    *(u32x4*)dst = c0;
    *(u32x4*)(dst + KIMG_B) = c1;
  }
  if (gw < 1376) {
    char* S = smem + K2_YOFF + w * 2048;
    *(u32x4*)(S + lane * 16) = wl[0];
    *(u32x4*)(S + 1024 + lane * 16) = wl[1];
    u32x4 o[2];
    int dst, dstr;
    if (gw < 688) {
#pragma unroll
      for (int f = 0; f < 2; ++f) {
        const char* a = S + (8 * g4 + qq) * 64 + f * 32 + p4 * 8;
        const u32x2 v0 = trread(a), v1 = trread(a + 4 * 64);
        o[f] = (u32x4){v0[0], v0[1], v1[0], v1[1]};
      }
      dst = ((2 * (gw >> 4)) * 16 + (gw & 15)) * 1024;
      dstr = 16 * 1024;
      float c2[2];
#pragma unroll
      for (int f = 0; f < 2; ++f) {
        c2[f] = ((bflo(o[f][0]) + bfhi(o[f][0])) + (bflo(o[f][1]) + bfhi(o[f][1]))) +
                ((bflo(o[f][2]) + bfhi(o[f][2])) + (bflo(o[f][3]) + bfhi(o[f][3])));
        c2[f] += __shfl_xor(c2[f], 16);
        c2[f] += __shfl_xor(c2[f], 32);
      }
      if (lane < 32) ((float*)(ws + OFF_CS))[(gw & 15) * F_ + 32 * (gw >> 4) + lane] = lane < 16 ? c2[0] : c2[1];
    } else {
#pragma unroll
      for (int f = 0; f < 2; ++f) {
        const char* a = S + (4 * g4 + qq) * 64 + p4 * 16 + f * 8;
        const u32x2 v0 = trread(a), v1 = trread(a + 16 * 64);
        o[f] = (u32x4){v0[0], v0[1], v1[0], v1[1]};
      }
      const int u2 = gw - 688, ap = u2 / KSF, kb = u2 - ap * KSF;
      dst = (int)(OFF_W2 - OFF_W1) + ((2 * ap) * KSF + kb) * 1024;
      dstr = KSF * 1024;
    }
    const __amdgpu_buffer_rsrc_t rw = __builtin_amdgcn_make_buffer_rsrc(ws + OFF_W1, 0, 4 << 20, 0x00020000);
    __builtin_amdgcn_raw_buffer_store_b128(o[0], rw, dst + lane * 16, 0, 0);
    __builtin_amdgcn_raw_buffer_store_b128(o[1], rw, dst + dstr + lane * 16, 0, 0);
  }
  SB();
  {
    char* ub_ = smem + K2_UOFF + c * K2_UCH + (v + 26) * 16;
    *(u32x4*)(ub_ + 0 * UCOPY_B) = (u32x4){ua[1], ua[2], ua[3], ub[0]};
    *(u32x4*)(ub_ + 1 * UCOPY_B) = (u32x4){ua[2], ua[3], ub[0], ub[1]};
    *(u32x4*)(ub_ + 2 * UCOPY_B) = (u32x4){ua[3], ub[0], ub[1], ub[2]};
    *(u32x4*)(ub_ + 3 * UCOPY_B) = ub;
    if (v == 0) {
      ub_ -= 16;
      *(u32x4*)(ub_ + 0 * UCOPY_B) = (u32x4){0u, 0u, 0u, ua[0]};
      *(u32x4*)(ub_ + 1 * UCOPY_B) = (u32x4){0u, 0u, ua[0], ua[1]};
      *(u32x4*)(ub_ + 2 * UCOPY_B) = (u32x4){0u, ua[0], ua[1], ua[2]};
      *(u32x4*)(ub_ + 3 * UCOPY_B) = ua;
    }
  }
  __syncthreads();
  if (role < 2) conv_main<0>(smem, ch, role & 1, lane);
  else conv_main<1>(smem, ch, role & 1, lane);
  __syncthreads();
  {
    const float* Y = (const float*)(smem + K2_Y2OFF) + c * 2048 + 8 * v;
    const f32x4 y0 = *(const f32x4*)Y + *(const f32x4*)(Y + 4096), y1 = *(const f32x4*)(Y + 4) + *(const f32x4*)(Y + 4100);
    u32x4 ho;
#pragma unroll
    for (int k = 0; k < 4; ++k) {
      const unsigned yb = k < 2 ? pack2(y0[2 * k], y0[2 * k + 1]) : pack2(y1[2 * k - 4], y1[2 * k - 3]);
      const unsigned sk = pack2(bflo(ua[k]) * dsk, bfhi(ua[k]) * dsk);
      const unsigned sm = pack2(bflo(yb) + bflo(sk), bfhi(yb) + bfhi(sk));
      ho[k] = pack2(bflo(xa[k]) + bflo(sm), bfhi(xa[k]) + bfhi(sm));
    }
    *(u32x4*)(ws + OFF_HT + (size_t)(v >> 3) * 65536 + (2 * blk + c) * 128 + (v & 7) * 16) = ho;
  }
  if (blk == 0 && t == 0) __hip_atomic_store((gu32*)(ws + OFF_EP), tag, RLX_AGENT);
}

constexpr int K3_HOFF = 0;
constexpr int K3_XOFF = 65536;
constexpr int K3_ROFF = 0;
constexpr int K3_SOFF = 131072;
constexpr int K3_COFF = K3_SOFF + 1024;
constexpr int K3_LDS = K3_COFF + 1536 + 6144;
constexpr int PF_LOOP = 1, PF_POST = 2;
constexpr int W1_PFD = 2;

__device__ __forceinline__ int k3_swz(int row) { return ((row >> 1) & 1) | (((row >> 3) & 1) << 1); }
__device__ __forceinline__ float gelu_t(float xx) {
  const float c0 = -2.0f * 0.7978845608028654f * 1.4426950408889634f;
  const float u = xx * __builtin_fmaf(xx * xx, c0 * 0.044715f, c0);
  return xx * __builtin_amdgcn_rcpf(1.0f + __builtin_amdgcn_exp2f(u));
}
__device__ __forceinline__ u32x2 gelu4(f32x4 a, const float* cb, float m, float sc) {
  const f32x4 cs = *(const f32x4*)cb, bv = *(const f32x4*)(cb + 192);
  const float o0 = gelu_t(sc * (a[0] - m * cs[0]) + bv[0]), o1 = gelu_t(sc * (a[1] - m * cs[1]) + bv[1]);
  const float o2 = gelu_t(sc * (a[2] - m * cs[2]) + bv[2]), o3 = gelu_t(sc * (a[3] - m * cs[3]) + bv[3]);
  return (u32x2){pack2(o0, o1), pack2(o2, o3)};
}

__global__ __launch_bounds__(512, 2) void k34_mlp(const u16* __restrict__ b1, const u16* __restrict__ b2,
                                                  char* __restrict__ ws, u16* __restrict__ out) {
  __shared__ __attribute__((aligned(16))) char smem[K3_LDS];
  const int b = blockIdx.x, xcd = b & 7, idx = b >> 3;
  const int rt = 4 * xcd + (idx >> 3), fg = idx & 7;
  const int fstart = fg < 6 ? 22 * (fg >> 1) + 12 * (fg & 1) : 66 + 10 * (fg - 6);
  const int cnt = (fg < 6 && !(fg & 1)) ? 12 : 10;
  const int t = threadIdx.x, lane = t & 63;
  const int w = __builtin_amdgcn_readfirstlane(t >> 6);
  const int fq = w & 3, kh = w >> 2;
  const bool has3 = 8 + fq < cnt;
  const int perm = 2 * kh + (fq & 1);
  const int row0 = rt * 64;
  const int dq = fg;
  const u16* hT = (const u16*)(ws + OFF_HT);
  const int oct = t & 7, dr = t >> 3;
  const int g = lane >> 4, li = lane & 15, qq = li >> 2, p = lane & 3;
  float* SP = (float*)(smem + K3_SOFF);
  const int r0 = 8 * g + qq;
  const int sw0 = k3_swz(r0) * 32;
  const char* base0 = smem + K3_HOFF + r0 * 128 + p * 8;
  const int T0 = fstart + 2 * fq, T2 = fstart + (has3 ? 8 + fq : 0);
  f32x4 acc[3][4] = {};
  u32x4 hres;
  unsigned epoch;
  const int pg = w < 3 ? 2 * w : w == 3 ? 1 : w == 4 ? 3 : w;
  const int kb0 = pg < 6 ? 11 * (pg >> 1) + 6 * (pg & 1) : 33 + 5 * (pg - 6);
  const bool six = pg < 6 && !(pg & 1);
  const u32x4* Ap = (const u32x4*)(ws + OFF_W2) + ((size_t)(4 * dq) * KSF + kb0) * 64 + lane;
  u32x4 av[6][4];
  const int k5 = six ? 5 : 4;
  {
    u32x4 hv[8];
#pragma unroll
    for (int i = 0; i < 8; ++i) hv[i] = *(const u32x4*)(hT + (size_t)rt * 32768 + (t + 512 * i) * 8);
    const u32x4* W1a = (const u32x4*)(ws + OFF_W1) + (T0 * 16 + 8 * kh) * 64 + lane;
    const u32x4* W1c = (const u32x4*)(ws + OFF_W1) + (T2 * 16 + 8 * kh) * 64 + lane;
    f32x4 cpart;
    u32x2 bpart;
    const int cf = min(16 * fstart + 4 * (t & 63), F_ - 4);
    {
      const float* cp = (const float*)(ws + OFF_CS) + (2 * w) * F_ + cf;
      cpart = *(const f32x4*)cp + *(const f32x4*)(cp + F_);
      bpart = *(const u32x2*)(b1 + cf);
    }
    epoch = __hip_atomic_load((gu32*)(ws + OFF_EP), RLX_AGENT);
    u32x4 wv[3][8];
    SB();
    char* hdst = smem + K3_HOFF + dr * 128 + (((oct >> 1) ^ k3_swz(dr)) * 32) + (oct & 1) * 16;
#pragma unroll
    for (int i = 0; i < 8; ++i) *(u32x4*)(hdst + i * (64 * 128)) = hv[i];
    if (lane < 48) {
      *(f32x4*)(smem + K3_COFF + 1536 + w * 768 + lane * 16) = cpart;
      if (w == 0) *(f32x4*)(smem + K3_COFF + 768 + lane * 16) = (f32x4){bflo(bpart[0]), bfhi(bpart[0]), bflo(bpart[1]), bfhi(bpart[1])};
    }
    SB();
#pragma unroll
    for (int ks = 0; ks < W1_PFD; ++ks) { wv[0][ks] = W1a[ks * 64]; wv[1][ks] = W1a[(16 + ks) * 64]; wv[2][ks] = W1c[ks * 64]; }
    SB();
    __syncthreads();
    if (w == 7 && lane < 48) {
      const f32x4* cp = (const f32x4*)(smem + K3_COFF + 1536) + lane;
      *(f32x4*)(smem + K3_COFF + lane * 16) = ((cp[0] + cp[48]) + (cp[96] + cp[144])) + ((cp[192] + cp[240]) + (cp[288] + cp[336]));
    }
    const u32x4 ones = {0x3f803f80u, 0x3f803f80u, 0x3f803f80u, 0x3f803f80u};
    f32x4 st0 = {}, st1 = {};
    const char* basek = base0 + kh * (8 * 4096);
    const char* basew = basek + ((fq * 32) ^ sw0);
    const int swp = sw0 ^ (perm * 32);
    bf16x8 B[2][4], fr[2];
    auto loadB = [&](int kk, int bf) {
#pragma unroll
      for (int lt = 0; lt < 4; ++lt) {
        const char* a = basek + kk * 4096 + ((lt * 32) ^ swp);
        const u32x2 v0 = trread(a), v1 = trread(a + 512);
        B[bf][lt] = asbf((u32x4){v0[0], v0[1], v1[0], v1[1]});
      }
      const u32x2 s0 = trread(basew + kk * 4096), s1 = trread(basew + kk * 4096 + 512);
      fr[bf] = asbf((u32x4){s0[0], s0[1], s1[0], s1[1]});
    };
    loadB(0, 0);
#pragma unroll
    for (int kk = 0; kk < 8; ++kk) {
      if (kk + W1_PFD < 8) {
        wv[0][kk + W1_PFD] = W1a[(kk + W1_PFD) * 64];
        wv[1][kk + W1_PFD] = W1a[(16 + kk + W1_PFD) * 64];
        wv[2][kk + W1_PFD] = W1c[(kk + W1_PFD) * 64];
      }
      if (kk >= 8 - PF_LOOP) {
#pragma unroll
        for (int i = 0; i < 4; ++i) av[kk - (8 - PF_LOOP)][i] = Ap[(i * KSF + kk - (8 - PF_LOOP)) * 64];
      }
      if (kk < 7) loadB(kk + 1, (kk + 1) & 1);
      SB();
      st0 = __builtin_amdgcn_mfma_f32_16x16x32_bf16(asbf(ones), fr[kk & 1], st0, 0, 0, 0);
      st1 = __builtin_amdgcn_mfma_f32_16x16x32_bf16(fr[kk & 1], fr[kk & 1], st1, 0, 0, 0);
#pragma unroll
      for (int n = 0; n < 3; ++n) {
#pragma unroll
        for (int lt = 0; lt < 4; ++lt)
          acc[n][lt] = __builtin_amdgcn_mfma_f32_16x16x32_bf16(asbf(wv[n][kk]), B[kk & 1][lt], acc[n][lt], 0, 0, 0);
      }
      SB();
    }
#pragma unroll
    for (int k = PF_LOOP; k < PF_POST; ++k)
#pragma unroll
      for (int i = 0; i < 4; ++i) av[k][i] = Ap[(i * KSF + k) * 64];
    SB();
    {
      const char* a = base0 + (2 * dq + (w & 1)) * 4096 + (((w >> 1) * 32) ^ sw0);
      const u32x2 v0 = trread(a), v1 = trread(a + 512);
      hres = (u32x4){v0[0], v0[1], v1[0], v1[1]};
    }
    if (g == qq) {
      SP[kh * 128 + 16 * fq + li] = st0[0];
      SP[kh * 128 + 64 + 16 * fq + li] = p == 0 ? st1[0] : p == 1 ? st1[1] : p == 2 ? st1[2] : st1[3];
    }
    f32x4* X = (f32x4*)(smem + K3_XOFF) + w * (8 * 64) + lane;
    const int o1 = (fq & 1) * 64;
    X[0 * 64 + o1] = acc[0][2]; X[1 * 64 - o1] = acc[0][3];
    X[2 * 64 + o1] = acc[1][2]; X[3 * 64 - o1] = acc[1][3];
#pragma unroll
    for (int lt = 1; lt < 4; ++lt) X[(4 + (lt ^ perm)) * 64] = acc[2][lt];
  }
  SB();
  __syncthreads();
  {
    const __amdgpu_buffer_rsrc_t rh = __builtin_amdgcn_make_buffer_rsrc(ws + OFF_HH, 0, L_ * F_ * 2, 0x00020000);
    const f32x4* XB = (const f32x4*)(smem + K3_XOFF) + lane;
    const f32x4* X = XB + (w ^ 4) * (8 * 64);
    const float* cb0 = (const float*)(smem + K3_COFF) + 32 * fq + 4 * g;
    float mm[2], ss[2];
#pragma unroll
    for (int h2 = 0; h2 < 2; ++h2) {
      const int l = 16 * (h2 ^ perm) + li;
      const float m = (SP[l] + SP[128 + l]) * (1.0f / D_);
      const float var = fmaxf((SP[64 + l] + SP[192 + l]) * (1.0f / D_) - m * m, 0.0f);
      const float r1 = __builtin_amdgcn_rsqf(var + 1e-5f);
      const float r2 = __builtin_amdgcn_rsqf(var * r1 * r1 + 1e-5f);
      mm[h2] = m; ss[h2] = r1 * r2;
    }
#pragma unroll
    for (int h2 = 0; h2 < 2; ++h2) {
      const int lt = h2 ^ perm, par = (h2 ^ fq) & 1;
      const u32x2 g0 = gelu4(acc[0][h2] + X[par * 64], cb0, mm[h2], ss[h2]);
      const u32x2 g1 = gelu4(acc[1][h2] + X[(2 + par) * 64], cb0 + 16, mm[h2], ss[h2]);
      __builtin_amdgcn_raw_buffer_store_b128((u32x4){g0[0], g0[1], g1[0], g1[1]}, rh,
                                             (((4 * rt + lt) * KSF + (T0 >> 1)) * 64 + lane) * 16, 0, 16);
      if (PF_POST + h2 < 6) {
#pragma unroll
        for (int i = 0; i < 4; ++i) av[PF_POST + h2][i] = Ap[(i * KSF + (PF_POST + h2 < 5 ? PF_POST + h2 : k5)) * 64];
      }
      SB();
    }
    if (has3) {
      const f32x4* Xo0 = XB + (w ^ 1) * (8 * 64);
      const f32x4* Xo1 = XB + (w ^ 5) * (8 * 64);
      const f32x4 aown = acc[2][0] + X[(4 + perm) * 64], aoth = Xo0[(4 + perm) * 64] + Xo1[(4 + perm) * 64];
      const bool odd = fq & 1;
      const float* cb2 = (const float*)(smem + K3_COFF) + 16 * (8 + (fq & 2)) + 4 * g;
      const u32x2 g0 = gelu4(odd ? aoth : aown, cb2, mm[0], ss[0]);
      const u32x2 g1 = gelu4(odd ? aown : aoth, cb2 + 16, mm[0], ss[0]);
      __builtin_amdgcn_raw_buffer_store_b128((u32x4){g0[0], g0[1], g1[0], g1[1]}, rh,
                                             (((4 * rt + perm) * KSF + (T2 >> 1)) * 64 + lane) * 16, 0, 16);
    }
  }
  asm volatile("s_waitcnt vmcnt(0)" ::: "memory");
  __builtin_amdgcn_s_barrier();
  gu64* flags = (gu64*)(ws + OFF_FL) + rt * (8 * 16);
  const unsigned tag = __builtin_amdgcn_readfirstlane(epoch);
  const unsigned long long fval = ((unsigned long long)tag << 32) | (unsigned)~tag;
  if (t == 0) __hip_atomic_store(flags + fg * 16, fval, RLX_AGENT);
#pragma unroll
  for (int k = PF_POST + 2; k < 6; ++k)
#pragma unroll
    for (int i = 0; i < 4; ++i) av[k][i] = Ap[(i * KSF + (k < 5 ? k : k5)) * 64];
  const u32x4 bias = *(const u32x4*)(b2 + 64 * dq + 32 * (w & 1) + 8 * g);
  SB();
  for (unsigned spins = 0; spins < (1u << 18); ++spins) {
    if (__all(__hip_atomic_load(flags + pg * 16, RLX_AGENT) == fval)) break;
    __builtin_amdgcn_s_sleep(1);
  }
  __builtin_amdgcn_fence(__ATOMIC_ACQUIRE, "wavefront");
  f32x4 oc[4][4] = {};
  {
    const __amdgpu_buffer_rsrc_t rs = __builtin_amdgcn_make_buffer_rsrc(ws + OFF_HH, 0, L_ * F_ * 2, 0x00020000);
    int boff = (((4 * rt) * KSF + kb0) * 64 + lane) * 16;
    asm volatile("" : "+v"(boff)::"memory");
    constexpr int NB = 3;
    u32x4 bv[NB][4];
#pragma unroll
    for (int k = 0; k < NB; ++k)
#pragma unroll
      for (int i = 0; i < 4; ++i) bv[k][i] = __builtin_amdgcn_raw_buffer_load_b128(rs, boff + (i * KSF + k) * 1024, 0, 16);
    SB();
#pragma unroll
    for (int k = 0; k < 6; ++k) {
      if (k < 5 || six) {
#pragma unroll
        for (int a = 0; a < 4; ++a)
#pragma unroll
          for (int bb = 0; bb < 4; ++bb)
            oc[a][bb] = __builtin_amdgcn_mfma_f32_16x16x32_bf16(asbf(av[k][a]), asbf(bv[k % NB][bb]), oc[a][bb], 0, 0, 0);
      }
      if (k + NB < 6) {
        const int kn = k + NB < 5 ? k + NB : k5;
#pragma unroll
        for (int i = 0; i < 4; ++i)
          bv[k % NB][i] = __builtin_amdgcn_raw_buffer_load_b128(rs, boff + (i * KSF + kn) * 1024, 0, 16);
      }
      SB();
    }
  }
  f32x4* R = (f32x4*)(smem + K3_ROFF);
#pragma unroll
  for (int bb = 0; bb < 4; ++bb)
#pragma unroll
    for (int a = 0; a < 4; ++a) R[((w * 4 + bb) * 4 + a) * 64 + lane] = oc[a][bb];
  __syncthreads();
  f32x4 sum[2];
#pragma unroll
  for (int a = 0; a < 2; ++a) {
    const f32x4* Rp = R + (((w >> 1)) * 4 + 2 * (w & 1) + a) * 64 + lane;
    f32x4 s0 = Rp[0 * 1024] + Rp[1 * 1024], s1 = Rp[2 * 1024] + Rp[3 * 1024];
    f32x4 s2 = Rp[4 * 1024] + Rp[5 * 1024], s3 = Rp[6 * 1024] + Rp[7 * 1024];
    sum[a] = (s0 + s1) + (s2 + s3);
  }
  const int lrow = row0 + 16 * (w >> 1) + li;
  u32x4 o;
#pragma unroll
  for (int hf = 0; hf < 2; ++hf) {
#pragma unroll
    for (int k = 0; k < 2; ++k) {
      const unsigned hb = hres[2 * hf + k], bb = bias[2 * hf + k];
      o[2 * hf + k] = pack2(bflo(hb) + (sum[hf][2 * k] + bflo(bb)), bfhi(hb) + (sum[hf][2 * k + 1] + bfhi(bb)));
    }
  }
  *(u32x4*)(out + (size_t)lrow * D_ + 64 * dq + 32 * (w & 1) + 8 * g) = o;
}

extern "C" void kernel_launch(void* const* d_in, const int* in_sizes, int n_in, void* d_out, int out_size, void* d_ws,
                              size_t ws_size, hipStream_t stream) {
  const u16* x = (const u16*)d_in[0];
  const u16* la = (const u16*)d_in[1];
  const u16* ai = (const u16*)d_in[2];
  const u16* cr = (const u16*)d_in[3];
  const u16* ci = (const u16*)d_in[4];
  const u16* ldt = (const u16*)d_in[5];
  const u16* dsk = (const u16*)d_in[6];
  const u16* w1 = (const u16*)d_in[8];
  const u16* b1 = (const u16*)d_in[9];
  const u16* w2 = (const u16*)d_in[10];
  const u16* b2 = (const u16*)d_in[11];
  char* ws = (char*)d_ws;
  k2_conv<<<256, 512, 0, stream>>>(x, la, ai, cr, ci, ldt, dsk, w1, w2, ws);
  k34_mlp<<<256, 512, 0, stream>>>(b1, b2, ws, (u16*)d_out);
}
```
